```python
import torch

def _softmax(x, axis=-1):
    return torch.softmax(x, dim=axis)
def _gelu(x):
    return torch.nn.functional.gelu(x, approximate="tanh")
def _silu(x):
    return torch.nn.functional.silu(x)
def _sigmoid(x):
    return torch.sigmoid(x)
def _layernorm(x, axis=-1, eps=1e-5):
    m = x.mean(dim=axis, keepdim=True)
    v = x.var(dim=axis, keepdim=True, unbiased=False)
    return (x - m) / torch.sqrt(v + eps)
def _scatter_add(tgt, idx, src, axis=0):
    return tgt.clone().index_add_(axis, idx, src)
def _segment_sum(src, seg, n):
    out = torch.zeros((n,) + src.shape[1:], dtype=src.dtype)
    return out.index_add_(0, seg, src)
def _pad(x, pw):
    flat = tuple(v for pair in reversed(pw) for v in pair)
    return torch.nn.functional.pad(x, flat)

kernel_name = 'standalone__gqa_block_cached__near_ridge'

def setup_inputs():

    _w = (lambda a: (lambda t: t.to(torch.bfloat16) if t.is_floating_point() else t)(torch.as_tensor(a)))
    B, Sq, D = 16, 32, 576
    Hq, Hkv, Sk, Dh = 32, 16, 32, 128
    assert Hq % Hkv == 0, (Hq, Hkv)
    return {
        "x": _w((lambda *s: torch.randn(*s, dtype=torch.bfloat16) * 0.8)(B, Sq, D)),
        "k_cache": _w((lambda *s: torch.randn(*s, dtype=torch.bfloat16) * 0.8)(B, Hkv, Sk, Dh)),
        "v_cache": _w((lambda *s: torch.randn(*s, dtype=torch.bfloat16) * 0.05)(B, Hkv, Sk, Dh)),
        "wq": _w((lambda *s: torch.randn(*s, dtype=torch.bfloat16) * 0.8)(D, Hq * Dh) * (D ** -0.5)),
        "wo": _w((lambda *s: torch.randn(*s, dtype=torch.bfloat16) * 0.05)(Hq * Dh, D) * ((Hq * Dh) ** -0.5)),
    }

def reference(x, k_cache, v_cache, wq, wo):
    B, Sq, D = x.shape
    Hq, Dh = 32, 128
    Hkv = k_cache.shape[1]
    rep = Hq // Hkv
    q = (lambda x, *d: x.permute(*d))((x @ wq).reshape(B, Sq, Hq, Dh), 0, 2, 1, 3)
    k = (lambda x, n, axis: torch.repeat_interleave(x, n, dim=axis))(k_cache, rep, axis=1)
    v = (lambda x, n, axis: torch.repeat_interleave(x, n, dim=axis))(v_cache, rep, axis=1)
    scores = torch.einsum("bhqd,bhkd->bhqk", q, k) * (Dh ** -0.5)
    p = _softmax(scores, axis=-1)
    o = torch.einsum("bhqk,bhkd->bhqd", p, v)
    return (lambda x, *d: x.permute(*d))(o, 0, 2, 1, 3).reshape(B, Sq, Hq * Dh) @ wo
```

```cpp
#include <hip/hip_runtime.h>
#include <hip/hip_bf16.h>
typedef unsigned short u16;
static __device__ __forceinline__ float bf2f(u16 v){ return __uint_as_float(((unsigned)v)<<16); }
static __device__ __forceinline__ u16 f2bf(float f){ return __builtin_bit_cast(u16,(__bf16)f); }

__global__ __launch_bounds__(256) void k1(const u16* x, const u16* kc, const u16* vc, const u16* wq, u16* o){
  __shared__ float q[32][129];
  __shared__ float p[32][33];
  int b = blockIdx.x>>5, h = blockIdx.x&31, g=h>>1;
  int t = threadIdx.x;
  int col = t&127, r0 = (t>>7)*16;
  float acc[16];
  #pragma unroll
  for(int i=0;i<16;i++) acc[i]=0;
  for(int k=0;k<576;k++){
    float w = bf2f(wq[k*4096+h*128+col]);
    #pragma unroll
    for(int i=0;i<16;i++) acc[i]+= bf2f(x[(b*32+r0+i)*576+k])*w;
  }
  #pragma unroll
  for(int i=0;i<16;i++) q[r0+i][col]=bf2f(f2bf(acc[i]));
  __syncthreads();
  for(int e=t;e<1024;e+=256){
    int i=e>>5,j=e&31; float s=0;
    for(int d=0;d<128;d++) s+=q[i][d]*bf2f(kc[((b*16+g)*32+j)*128+d]);
    p[i][j]=s*0.08838834764831845f;
  }
  __syncthreads();
  if(t<32){ float m=-1e30f; for(int j=0;j<32;j++) m=fmaxf(m,p[t][j]); float s=0; for(int j=0;j<32;j++){ float e=__expf(p[t][j]-m); p[t][j]=e; s+=e;} for(int j=0;j<32;j++) p[t][j]=bf2f(f2bf(p[t][j]/s)); }
  __syncthreads();
  #pragma unroll
  for(int i=0;i<16;i++){ float s=0; for(int j=0;j<32;j++) s+=p[r0+i][j]*bf2f(vc[((b*16+g)*32+j)*128+col]); o[(b*32+r0+i)*4096+h*128+col]=f2bf(s);}
}
__global__ __launch_bounds__(64) void k2(const u16* o, const u16* wo, u16* out){
  int n = blockIdx.x*64+threadIdx.x; int m0 = blockIdx.y*8;
  float acc[8];
  #pragma unroll
  for(int i=0;i<8;i++) acc[i]=0;
  for(int k=0;k<4096;k++){ float w=bf2f(wo[k*576+n]);
    #pragma unroll
    for(int i=0;i<8;i++) acc[i]+=bf2f(o[(m0+i)*4096+k])*w; }
  #pragma unroll
  for(int i=0;i<8;i++) out[(m0+i)*576+n]=f2bf(acc[i]);
}
extern "C" void kernel_launch(void* const* d_in, const int* in_sizes, int n_in, void* d_out, int out_size, void* d_ws, size_t ws_size, hipStream_t stream){
  const u16* x=(const u16*)d_in[0]; const u16* kc=(const u16*)d_in[1]; const u16* vc=(const u16*)d_in[2]; const u16* wq=(const u16*)d_in[3]; const u16* wo=(const u16*)d_in[4];
  u16* o=(u16*)d_ws;
  k1<<<512,256,0,stream>>>(x,kc,vc,wq,o);
  k2<<<dim3(9,64),64,0,stream>>>(o,wo,(u16*)d_out);
}
```

```cpp
#include <hip/hip_runtime.h>
#include <hip/hip_bf16.h>
typedef unsigned short u16;
static __device__ __forceinline__ float bf2f(u16 v){ return __uint_as_float(((unsigned)v)<<16); }
static __device__ __forceinline__ u16 f2bf(float f){ return __builtin_bit_cast(u16,(__bf16)f); }

__global__ __launch_bounds__(256) void k1(const u16* x, const u16* kc, const u16* vc, const u16* wq, u16* o){
  __shared__ float q[32][129];
  __shared__ float p[32][33];
  int b = blockIdx.x>>5, h = blockIdx.x&31, g=h>>1;
  int t = threadIdx.x;
  int col = t&127, r0 = (t>>7)*16;
  float acc[16];
  #pragma unroll
  for(int i=0;i<16;i++) acc[i]=0;
  for(int k=0;k<576;k++){
    float w = bf2f(wq[k*4096+h*128+col]);
    #pragma unroll
    for(int i=0;i<16;i++) acc[i]+= bf2f(x[(b*32+r0+i)*576+k])*w;
  }
  #pragma unroll
  for(int i=0;i<16;i++) q[r0+i][col]=bf2f(f2bf(acc[i]));
  __syncthreads();
  for(int e=t;e<1024;e+=256){
    int i=e>>5,j=e&31; float s=0;
    for(int d=0;d<128;d++) s+=q[i][d]*bf2f(kc[((b*16+g)*32+j)*128+d]);
    p[i][j]=s*0.08838834764831845f;
  }
  __syncthreads();
  if(t<32){ float m=-1e30f; for(int j=0;j<32;j++) m=fmaxf(m,p[t][j]); float s=0; for(int j=0;j<32;j++){ float e=__expf(p[t][j]-m); p[t][j]=e; s+=e;} for(int j=0;j<32;j++) p[t][j]=bf2f(f2bf(p[t][j]/s)); }
  __syncthreads();
  #pragma unroll
  for(int i=0;i<16;i++){ float s=0; for(int j=0;j<32;j++) s+=p[r0+i][j]*bf2f(vc[((b*16+g)*32+j)*128+col]); o[(b*32+r0+i)*4096+h*128+col]=f2bf(s);}
}
__global__ __launch_bounds__(64) void k2(const u16* o, const u16* wo, u16* out){
  int n = blockIdx.x*64+threadIdx.x; int m0 = blockIdx.y*8;
  float acc[8];
  #pragma unroll
  for(int i=0;i<8;i++) acc[i]=0;
  for(int k=0;k<4096;k++){ float w=bf2f(wo[k*576+n]);
    #pragma unroll
    for(int i=0;i<8;i++) acc[i]+=bf2f(o[(m0+i)*4096+k])*w; }
  #pragma unroll
  for(int i=0;i<8;i++) out[(m0+i)*576+n]=f2bf(acc[i]);
}

typedef unsigned v4u __attribute__((ext_vector_type(4)));
#define RT() __builtin_amdgcn_s_memrealtime()
__device__ __forceinline__ void spin_until(unsigned long long t){ while(RT()<t) __builtin_amdgcn_s_sleep(8); }

__global__ __launch_bounds__(512) void probe(const char* x,const char* kc,const char* vc,const char* wq,const char* wo,char* ws){
  unsigned long long t0=RT();
  int b=blockIdx.x, t=threadIdx.x; v4u dd={0,0,0,0};
  const char* p;
  p=wq+(size_t)b*18432+t*16; asm volatile("global_load_dwordx4 %0, %1, off":"+v"(dd):"v"(p):"memory");
  p+=8192; asm volatile("global_load_dwordx4 %0, %1, off":"+v"(dd):"v"(p):"memory");
  p=wo+(size_t)b*18432+t*16; asm volatile("global_load_dwordx4 %0, %1, off":"+v"(dd):"v"(p):"memory");
  p+=8192; asm volatile("global_load_dwordx4 %0, %1, off":"+v"(dd):"v"(p):"memory");
  p=kc+(size_t)b*8192+t*16; asm volatile("global_load_dwordx4 %0, %1, off":"+v"(dd):"v"(p):"memory");
  p=vc+(size_t)b*8192+t*16; asm volatile("global_load_dwordx4 %0, %1, off":"+v"(dd):"v"(p):"memory");
  if(t<128){ p=wq+(size_t)b*18432+16384+t*16; asm volatile("global_load_dwordx4 %0, %1, off":"+v"(dd):"v"(p):"memory");
             p=wo+(size_t)b*18432+16384+t*16; asm volatile("global_load_dwordx4 %0, %1, off":"+v"(dd):"v"(p):"memory");
             p=x+(size_t)b*2304+t*16; asm volatile("global_load_dwordx4 %0, %1, off":"+v"(dd):"v"(p):"memory"); }
  asm volatile("s_waitcnt vmcnt(0)":"+v"(dd)::"memory");
  unsigned long long t1=RT();
  spin_until(t0+(t1-t0)*100);
}
extern "C" void kernel_launch(void* const* d_in, const int* in_sizes, int n_in, void* d_out, int out_size, void* d_ws, size_t ws_size, hipStream_t stream){
  const u16* x=(const u16*)d_in[0]; const u16* kc=(const u16*)d_in[1]; const u16* vc=(const u16*)d_in[2]; const u16* wq=(const u16*)d_in[3]; const u16* wo=(const u16*)d_in[4];
  u16* o=(u16*)d_ws;
  probe<<<256,512,0,stream>>>((const char*)x,(const char*)kc,(const char*)vc,(const char*)wq,(const char*)wo,(char*)d_ws);
  k1<<<512,256,0,stream>>>(x,kc,vc,wq,o);
  k2<<<dim3(9,64),64,0,stream>>>(o,wo,(u16*)d_out);
}
```

```cpp
#include <hip/hip_runtime.h>
#include <hip/hip_bf16.h>
typedef unsigned short u16;
static __device__ __forceinline__ float bf2f(u16 v){ return __uint_as_float(((unsigned)v)<<16); }
static __device__ __forceinline__ u16 f2bf(float f){ return __builtin_bit_cast(u16,(__bf16)f); }

__global__ __launch_bounds__(256) void k1(const u16* x, const u16* kc, const u16* vc, const u16* wq, u16* o){
  __shared__ float q[32][129];
  __shared__ float p[32][33];
  int b = blockIdx.x>>5, h = blockIdx.x&31, g=h>>1;
  int t = threadIdx.x;
  int col = t&127, r0 = (t>>7)*16;
  float acc[16];
  #pragma unroll
  for(int i=0;i<16;i++) acc[i]=0;
  for(int k=0;k<576;k++){
    float w = bf2f(wq[k*4096+h*128+col]);
    #pragma unroll
    for(int i=0;i<16;i++) acc[i]+= bf2f(x[(b*32+r0+i)*576+k])*w;
  }
  #pragma unroll
  for(int i=0;i<16;i++) q[r0+i][col]=bf2f(f2bf(acc[i]));
  __syncthreads();
  for(int e=t;e<1024;e+=256){
    int i=e>>5,j=e&31; float s=0;
    for(int d=0;d<128;d++) s+=q[i][d]*bf2f(kc[((b*16+g)*32+j)*128+d]);
    p[i][j]=s*0.08838834764831845f;
  }
  __syncthreads();
  if(t<32){ float m=-1e30f; for(int j=0;j<32;j++) m=fmaxf(m,p[t][j]); float s=0; for(int j=0;j<32;j++){ float e=__expf(p[t][j]-m); p[t][j]=e; s+=e;} for(int j=0;j<32;j++) p[t][j]=bf2f(f2bf(p[t][j]/s)); }
  __syncthreads();
  #pragma unroll
  for(int i=0;i<16;i++){ float s=0; for(int j=0;j<32;j++) s+=p[r0+i][j]*bf2f(vc[((b*16+g)*32+j)*128+col]); o[(b*32+r0+i)*4096+h*128+col]=f2bf(s);}
}
__global__ __launch_bounds__(64) void k2(const u16* o, const u16* wo, u16* out){
  int n = blockIdx.x*64+threadIdx.x; int m0 = blockIdx.y*8;
  float acc[8];
  #pragma unroll
  for(int i=0;i<8;i++) acc[i]=0;
  for(int k=0;k<4096;k++){ float w=bf2f(wo[k*576+n]);
    #pragma unroll
    for(int i=0;i<8;i++) acc[i]+=bf2f(o[(m0+i)*4096+k])*w; }
  #pragma unroll
  for(int i=0;i<8;i++) out[(m0+i)*576+n]=f2bf(acc[i]);
}

typedef unsigned v4u __attribute__((ext_vector_type(4)));
#define RT() __builtin_amdgcn_s_memrealtime()
__device__ __forceinline__ void spin_until(unsigned long long t){ while(RT()<t) __builtin_amdgcn_s_sleep(8); }

__global__ __launch_bounds__(256) void probe(const char* x,const char* kc,const char* vc,const char* wq,const char* wo,char* ws){
  unsigned long long ka=(unsigned long long)__builtin_amdgcn_kernarg_segment_ptr();
  unsigned long long t0;
  asm volatile("s_memrealtime %0\n s_waitcnt lgkmcnt(0)":"=s"(t0)::"memory");
  unsigned long long a;
  asm volatile("s_load_dwordx2 %0, %1, 0x0\n s_waitcnt lgkmcnt(0)":"=s"(a):"s"(ka):"memory");
  unsigned long long t1;
  asm volatile("s_memrealtime %0\n s_waitcnt lgkmcnt(0)":"=s"(t1):"s"(a):"memory");
  spin_until(t0+(t1-t0)*200);
}
extern "C" void kernel_launch(void* const* d_in, const int* in_sizes, int n_in, void* d_out, int out_size, void* d_ws, size_t ws_size, hipStream_t stream){
  const u16* x=(const u16*)d_in[0]; const u16* kc=(const u16*)d_in[1]; const u16* vc=(const u16*)d_in[2]; const u16* wq=(const u16*)d_in[3]; const u16* wo=(const u16*)d_in[4];
  u16* o=(u16*)d_ws;
  probe<<<256,256,0,stream>>>((const char*)x,(const char*)kc,(const char*)vc,(const char*)wq,(const char*)wo,(char*)d_ws);
  k1<<<512,256,0,stream>>>(x,kc,vc,wq,o);
  k2<<<dim3(9,64),64,0,stream>>>(o,wo,(u16*)d_out);
}
```

```cpp
#include <hip/hip_runtime.h>
#include <hip/hip_bf16.h>

__global__ __launch_bounds__(512, 2) void gqa_block_kernel(
    const void* x, unsigned xn, unsigned xf, const void* k, unsigned kn, unsigned kf,
    const void* v, unsigned vn, unsigned vf, const void* wq, unsigned wqn, unsigned wqf,
    const void* wo, unsigned won, unsigned wof, void* ws, unsigned wsn, unsigned wsf,
    void* out, unsigned outn, unsigned outf) {
  __shared__ __attribute__((aligned(16))) unsigned char lds[163840];
  asm volatile(
    ".Lcode0:\n"
    "s_load_dwordx16 s[8:23], %0, 0\n"
    "s_load_dwordx8 s[24:31], %0, 64\n"
    "s_load_dwordx4 s[32:35], %0, 96\n"
    "v_readfirstlane_b32 s36, v0\n"
    "v_and_b32 v0, 63, v0\n"
    "s_and_b32 s37, s2, 7\n"
    "s_lshr_b32 s43, s2, 4\n"
    "s_lshr_b32 s36, s36, 6\n"
    "s_and_b32 s38, s43, 3\n"
    "s_lshr_b32 s39, s43, 2\n"
    "s_and_b32 s41, s36, 3\n"
    "v_lshrrev_b32 v22, 4, v0\n"
    "v_and_b32 v23, 15, v0\n"
    "s_cmp_lt_u32 s36, 4\n"
    "s_cbranch_scc1 .Lload\n"
    "s_getpc_b64 s[44:45]\n"
    "s_lshr_b32 s42, s2, 3\n"
    "s_lshl2_add_u32 s42, s42, s41\n"
    "s_min_u32 s46, s42, ((.Lcode1-.Lcode0)>>7)+1\n"
    "s_lshl_b32 s46, s46, 7\n"
    "s_and_b32 s44, s44, 0xffffff80\n"
    "s_load_dwordx2 s[58:59], s[44:45], s46 offset:60\n"
    "s_add_u32 s42, s42, 128\n"
    "s_min_u32 s46, s42, ((.Lcode1-.Lcode0)>>7)+1\n"
    "s_lshl_b32 s46, s46, 7\n"
    "s_load_dwordx2 s[58:59], s[44:45], s46 offset:60\n"
    "s_branch .Lmfma\n"
    ".Lload:\n"
    "s_bitcmp1_b32 s2, 3\n"
    "s_cbranch_scc1 .Lr2l\n"
    "v_lshrrev_b32 v26, 3, v0\n"
    "v_and_b32 v25, 7, v0\n"
    "v_xor_b32 v27, v22, v25\n"
    "v_lshlrev_b32 v27, 4, v27\n"
    "v_mul_u32_u24 v26, 1152, v26\n"
    "v_add_u32 v14, v26, v27\n"
    "v_xor_b32 v15, 64, v14\n"
    "v_add_u32 v16, 16384, v14\n"
    "v_add_u32 v17, 24576, v15\n"
    "v_add_u32 v15, 8192, v15\n"
    "s_lshl2_add_u32 s53, s39, s41\n"
    "s_mul_i32 s47, s53, 36864\n"
    "s_lshl_b32 s48, s41, 12\n"
    "s_add_u32 s49, s48, 16384\n"
    "v_lshlrev_b32 v24, 2, v22\n"
    "v_xor_b32 v24, v24, v23\n"
    "v_lshlrev_b32 v24, 4, v24\n"
    "v_lshl_or_b32 v10, v22, 13, v24\n"
    "v_add_u32 v11, 31744, v10\n"
    "v_xor_b32 v13, 32, v10\n"
    "v_add_u32 v12, 63488, v13\n"
    "v_add_u32 v13, 95232, v13\n"
    "s_lshl_b32 s46, s41, 9\n"
    "s_lshl2_add_u32 s46, s37, s46\n"
    "s_add_u32 s46, s46, s38\n"
    "s_lshl_b32 s46, s46, 8\n"
    "s_mov_b32 s51, 0\n"
    "s_mov_b32 s52, 9\n"
    "s_waitcnt lgkmcnt(0)\n"
    "s_branch .Lr1l_in\n"
    ".Lr1l_loop:\n"
    "s_waitcnt vmcnt(8)\n"
    "s_barrier\n"
    ".Lr1l_in:\n"
    "s_add_u32 m0, s49, s51\n"
    "s_sub_u32 s52, s52, 1\n"
    "buffer_load_dwordx4 v14, s[8:11], s47 offen offset:0 lds\n"
    "buffer_load_dwordx4 v15, s[8:11], s47 offen offset:1024 lds\n"
    "buffer_load_dwordx4 v16, s[8:11], s47 offen offset:2048 lds\n"
    "buffer_load_dwordx4 v17, s[8:11], s47 offen offset:3072 lds\n"
    "s_add_u32 m0, s48, s51\n"
    "s_add_u32 s47, s47, 128\n"
    "buffer_load_dwordx4 v10, s[20:23], s46 offen offset:0 lds\n"
    "buffer_load_dwordx4 v11, s[20:23], s46 offen offset:1024 lds\n"
    "buffer_load_dwordx4 v12, s[20:23], s46 offen offset:2048 lds\n"
    "buffer_load_dwordx4 v13, s[20:23], s46 offen offset:3072 lds\n"
    "s_add_u32 s46, s46, 0x80000\n"
    "s_add_u32 s51, s51, 0x8000\n"
    "s_and_b32 s51, s51, 0x1ffff\n"
    "s_cmp_eq_u32 s52, 8\n"
    "s_cbranch_scc1 .Lr1l_in\n"
    "s_cmp_eq_u32 s52, 7\n"
    "s_cbranch_scc1 .Lr1l_setup\n"
    "s_cmp_lg_u32 s52, 0\n"
    "s_cbranch_scc1 .Lr1l_loop\n"
    "s_branch .Lr1l_tail\n"
    ".Lr2l:\n"
    "s_cmp_ge_u32 s41, 2\n"
    "s_cbranch_scc1 .Lr2h\n"
    "v_lshlrev_b32 v25, 1, v0\n"
    "v_and_b32 v25, 64, v25\n"
    "v_bfe_u32 v26, v0, 3, 2\n"
    "v_or_b32 v25, v25, v26\n"
    "v_and_b32 v26, 7, v0\n"
    "v_lshlrev_b32 v26, 7, v26\n"
    "v_lshl_or_b32 v14, v25, 13, v26\n"
    "s_lshl_b32 s46, s43, 5\n"
    "s_add_u32 s46, s46, s37\n"
    "s_lshl_b32 s46, s46, 10\n"
    "s_sub_u32 s7, 0, s41\n"
    "s_not_b32 s6, s7\n"
    "v_lshrrev_b32 v25, 1, v0\n"
    "v_mul_u32_u24 v25, 1152, v25\n"
    "v_and_b32 v27, 1, v0\n"
    "v_lshl_add_u32 v15, v27, 7, v25\n"
    "s_mul_i32 s47, s43, 36864\n"
    "s_waitcnt lgkmcnt(0)\n"
    "s_mov_b64 exec, s[6:7]\n"
    "buffer_load_dword v30, v14, s[20:23], s46 offen\n"
    "s_sleep 4\n"
    "buffer_load_dword v30, v15, s[8:11], s47 offen offset:128\n"
    "s_sleep 4\n"
    "s_add_u32 s42, s46, 1048576\n"
    "buffer_load_dword v30, v14, s[20:23], s42 offen\n"
    "s_sleep 4\n"
    "buffer_load_dword v30, v15, s[8:11], s47 offen offset:384\n"
    "s_add_u32 s42, s46, 2097152\n"
    "buffer_load_dword v30, v14, s[20:23], s42 offen\n"
    "buffer_load_dword v30, v15, s[8:11], s47 offen offset:640\n"
    "s_add_u32 s42, s46, 3145728\n"
    "buffer_load_dword v30, v14, s[20:23], s42 offen\n"
    "buffer_load_dword v30, v15, s[8:11], s47 offen offset:896\n"
    "s_add_u32 s42, s46, 4194304\n"
    "buffer_load_dword v30, v14, s[20:23], s42 offen\n"
    "s_mov_b64 exec, -1\n"
    "v_lshlrev_b32 v16, 7, v0\n"
    "v_lshrrev_b32 v27, 3, v0\n"
    "v_mul_u32_u24 v27, 1152, v27\n"
    "v_add_u32 v17, v27, v26\n"
    "s_lshl_b32 s42, s43, 4\n"
    "s_lshl1_add_u32 s42, s37, s42\n"
    "s_lshl_b32 s48, s42, 13\n"
    "s_lshl_b32 s42, s37, 6\n"
    "s_add_u32 s42, s42, s43\n"
    "s_mul_i32 s49, s42, 9216\n"
    "s_lshl4_add_u32 s42, s37, s43\n"
    "s_mul_i32 s50, s42, 36864\n"
    "v_lshlrev_b32 v24, 2, v22\n"
    "v_xor_b32 v24, v24, v23\n"
    "v_lshlrev_b32 v24, 4, v24\n"
    "v_mul_u32_u24 v10, 1152, v22\n"
    "v_add_u32 v10, v10, v24\n"
    "v_add_u32 v11, 3584, v10\n"
    "v_xor_b32 v13, 32, v10\n"
    "v_add_u32 v12, 7168, v13\n"
    "v_add_u32 v13, 10752, v13\n"
    "v_xor_b32 v23, v22, v23\n"
    "v_lshlrev_b32 v23, 4, v23\n"
    "v_lshl_or_b32 v18, v22, 8, v23\n"
    "v_xor_b32 v19, 64, v18\n"
    "v_xor_b32 v20, 0x80, v18\n"
    "v_xor_b32 v21, 0xc0, v18\n"
    "s_lshl_b32 s56, s41, 12\n"
    "s_lshl_b32 s42, s39, 6\n"
    "s_lshl1_add_u32 s42, s37, s42\n"
    "s_lshl_b32 s54, s42, 13\n"
    "s_add_u32 s54, s54, s56\n"
    "s_lshl_b32 s42, s37, 5\n"
    "s_add_u32 s42, s42, s41\n"
    "s_mul_i32 s55, s42, 18432\n"
    "s_lshl_b32 s42, s38, 8\n"
    "s_add_u32 s55, s55, s42\n"
    "s_lshl_b32 s42, s2, 6\n"
    "s_buffer_load_dwordx2 s[96:97], s[28:31], s42 offset:24\n"
    "s_lshl_b32 s42, s38, 17\n"
    "s_add_u32 s42, s42, s54\n"
    "s_lshl_b32 s57, s38, 13\n"
    "s_add_u32 s57, s57, s56\n"
    "s_add_u32 m0, s57, 98304\n"
    "s_nop 0\n"
    "buffer_load_dwordx4 v18, s[16:19], s42 offen offset:0 lds\n"
    "buffer_load_dwordx4 v19, s[16:19], s42 offen offset:1024 lds\n"
    "buffer_load_dwordx4 v20, s[16:19], s42 offen offset:2048 lds\n"
    "buffer_load_dwordx4 v21, s[16:19], s42 offen offset:3072 lds\n"
    "s_mul_i32 s42, s39, 36864\n"
    "s_add_u32 s42, s42, s55\n"
    "s_lshl_b32 s57, s39, 13\n"
    "s_add_u32 s57, s57, s56\n"
    "s_add_u32 m0, s57, 0\n"
    "s_nop 0\n"
    "buffer_load_dwordx4 v10, s[24:27], s42 offen offset:0 lds\n"
    "buffer_load_dwordx4 v11, s[24:27], s42 offen offset:1024 lds\n"
    "buffer_load_dwordx4 v12, s[24:27], s42 offen offset:2048 lds\n"
    "buffer_load_dwordx4 v13, s[24:27], s42 offen offset:3072 lds\n"
    "s_mov_b64 exec, s[6:7]\n"
    "buffer_load_dword v30, v25, s[24:27], s50 offen offset:1024\n"
    "s_mov_b64 exec, -1\n"
    "s_mul_i32 s42, s39, 36864\n"
    "s_add_u32 s42, s42, s55\n"
    "s_add_u32 s42, s42, 147456\n"
    "s_lshl_b32 s57, s39, 13\n"
    "s_add_u32 s57, s57, s56\n"
    "s_add_u32 m0, s57, 32768\n"
    "s_nop 0\n"
    "buffer_load_dwordx4 v10, s[24:27], s42 offen offset:0 lds\n"
    "buffer_load_dwordx4 v11, s[24:27], s42 offen offset:1024 lds\n"
    "buffer_load_dwordx4 v12, s[24:27], s42 offen offset:2048 lds\n"
    "buffer_load_dwordx4 v13, s[24:27], s42 offen offset:3072 lds\n"
    "s_lshl_b32 s42, s38, 17\n"
    "s_add_u32 s42, s42, s54\n"
    "s_add_u32 s42, s42, 8192\n"
    "s_lshl_b32 s57, s38, 13\n"
    "s_add_u32 s57, s57, s56\n"
    "s_add_u32 m0, s57, 131072\n"
    "s_nop 0\n"
    "buffer_load_dwordx4 v18, s[16:19], s42 offen offset:0 lds\n"
    "buffer_load_dwordx4 v19, s[16:19], s42 offen offset:1024 lds\n"
    "buffer_load_dwordx4 v20, s[16:19], s42 offen offset:2048 lds\n"
    "buffer_load_dwordx4 v21, s[16:19], s42 offen offset:3072 lds\n"
    "s_mul_i32 s42, s39, 36864\n"
    "s_add_u32 s42, s42, s55\n"
    "s_add_u32 s42, s42, 294912\n"
    "s_lshl_b32 s57, s39, 13\n"
    "s_add_u32 s57, s57, s56\n"
    "s_add_u32 m0, s57, 65536\n"
    "s_nop 0\n"
    "buffer_load_dwordx4 v10, s[24:27], s42 offen offset:0 lds\n"
    "buffer_load_dwordx4 v11, s[24:27], s42 offen offset:1024 lds\n"
    "buffer_load_dwordx4 v12, s[24:27], s42 offen offset:2048 lds\n"
    "buffer_load_dwordx4 v13, s[24:27], s42 offen offset:3072 lds\n"
    "s_xor_b32 s57, s38, 1\n"
    "s_lshl_b32 s42, s57, 17\n"
    "s_add_u32 s42, s42, s54\n"
    "s_lshl_b32 s57, s57, 13\n"
    "s_add_u32 s57, s57, s56\n"
    "s_add_u32 m0, s57, 98304\n"
    "s_nop 0\n"
    "buffer_load_dwordx4 v18, s[16:19], s42 offen offset:0 lds\n"
    "buffer_load_dwordx4 v19, s[16:19], s42 offen offset:1024 lds\n"
    "buffer_load_dwordx4 v20, s[16:19], s42 offen offset:2048 lds\n"
    "buffer_load_dwordx4 v21, s[16:19], s42 offen offset:3072 lds\n"
    "s_xor_b32 s57, s39, 1\n"
    "s_mul_i32 s42, s57, 36864\n"
    "s_add_u32 s42, s42, s55\n"
    "s_lshl_b32 s57, s57, 13\n"
    "s_add_u32 s57, s57, s56\n"
    "s_add_u32 m0, s57, 0\n"
    "s_nop 0\n"
    "buffer_load_dwordx4 v10, s[24:27], s42 offen offset:0 lds\n"
    "buffer_load_dwordx4 v11, s[24:27], s42 offen offset:1024 lds\n"
    "buffer_load_dwordx4 v12, s[24:27], s42 offen offset:2048 lds\n"
    "buffer_load_dwordx4 v13, s[24:27], s42 offen offset:3072 lds\n"
    "s_xor_b32 s57, s38, 2\n"
    "s_lshl_b32 s42, s57, 17\n"
    "s_add_u32 s42, s42, s54\n"
    "s_lshl_b32 s57, s57, 13\n"
    "s_add_u32 s57, s57, s56\n"
    "s_add_u32 m0, s57, 98304\n"
    "s_nop 0\n"
    "buffer_load_dwordx4 v18, s[16:19], s42 offen offset:0 lds\n"
    "buffer_load_dwordx4 v19, s[16:19], s42 offen offset:1024 lds\n"
    "buffer_load_dwordx4 v20, s[16:19], s42 offen offset:2048 lds\n"
    "buffer_load_dwordx4 v21, s[16:19], s42 offen offset:3072 lds\n"
    "s_xor_b32 s57, s39, 2\n"
    "s_mul_i32 s42, s57, 36864\n"
    "s_add_u32 s42, s42, s55\n"
    "s_lshl_b32 s57, s57, 13\n"
    "s_add_u32 s57, s57, s56\n"
    "s_add_u32 m0, s57, 0\n"
    "s_nop 0\n"
    "buffer_load_dwordx4 v10, s[24:27], s42 offen offset:0 lds\n"
    "buffer_load_dwordx4 v11, s[24:27], s42 offen offset:1024 lds\n"
    "buffer_load_dwordx4 v12, s[24:27], s42 offen offset:2048 lds\n"
    "buffer_load_dwordx4 v13, s[24:27], s42 offen offset:3072 lds\n"
    "s_xor_b32 s57, s38, 3\n"
    "s_lshl_b32 s42, s57, 17\n"
    "s_add_u32 s42, s42, s54\n"
    "s_lshl_b32 s57, s57, 13\n"
    "s_add_u32 s57, s57, s56\n"
    "s_add_u32 m0, s57, 98304\n"
    "s_nop 0\n"
    "buffer_load_dwordx4 v18, s[16:19], s42 offen offset:0 lds\n"
    "buffer_load_dwordx4 v19, s[16:19], s42 offen offset:1024 lds\n"
    "buffer_load_dwordx4 v20, s[16:19], s42 offen offset:2048 lds\n"
    "buffer_load_dwordx4 v21, s[16:19], s42 offen offset:3072 lds\n"
    "s_xor_b32 s57, s39, 3\n"
    "s_mul_i32 s42, s57, 36864\n"
    "s_add_u32 s42, s42, s55\n"
    "s_lshl_b32 s57, s57, 13\n"
    "s_add_u32 s57, s57, s56\n"
    "s_add_u32 m0, s57, 0\n"
    "s_nop 0\n"
    "buffer_load_dwordx4 v10, s[24:27], s42 offen offset:0 lds\n"
    "buffer_load_dwordx4 v11, s[24:27], s42 offen offset:1024 lds\n"
    "buffer_load_dwordx4 v12, s[24:27], s42 offen offset:2048 lds\n"
    "buffer_load_dwordx4 v13, s[24:27], s42 offen offset:3072 lds\n"
    "s_xor_b32 s57, s39, 1\n"
    "s_mul_i32 s42, s57, 36864\n"
    "s_add_u32 s42, s42, s55\n"
    "s_add_u32 s42, s42, 147456\n"
    "s_lshl_b32 s57, s57, 13\n"
    "s_add_u32 s57, s57, s56\n"
    "s_add_u32 m0, s57, 32768\n"
    "s_nop 0\n"
    "buffer_load_dwordx4 v10, s[24:27], s42 offen offset:0 lds\n"
    "buffer_load_dwordx4 v11, s[24:27], s42 offen offset:1024 lds\n"
    "buffer_load_dwordx4 v12, s[24:27], s42 offen offset:2048 lds\n"
    "buffer_load_dwordx4 v13, s[24:27], s42 offen offset:3072 lds\n"
    "s_xor_b32 s57, s39, 2\n"
    "s_mul_i32 s42, s57, 36864\n"
    "s_add_u32 s42, s42, s55\n"
    "s_add_u32 s42, s42, 147456\n"
    "s_lshl_b32 s57, s57, 13\n"
    "s_add_u32 s57, s57, s56\n"
    "s_add_u32 m0, s57, 32768\n"
    "s_nop 0\n"
    "buffer_load_dwordx4 v10, s[24:27], s42 offen offset:0 lds\n"
    "buffer_load_dwordx4 v11, s[24:27], s42 offen offset:1024 lds\n"
    "buffer_load_dwordx4 v12, s[24:27], s42 offen offset:2048 lds\n"
    "buffer_load_dwordx4 v13, s[24:27], s42 offen offset:3072 lds\n"
    "s_waitcnt vmcnt(8)\n"
    "s_barrier\n"
    "s_xor_b32 s57, s39, 3\n"
    "s_mul_i32 s42, s57, 36864\n"
    "s_add_u32 s42, s42, s55\n"
    "s_add_u32 s42, s42, 147456\n"
    "s_lshl_b32 s57, s57, 13\n"
    "s_add_u32 s57, s57, s56\n"
    "s_add_u32 m0, s57, 32768\n"
    "s_nop 0\n"
    "buffer_load_dwordx4 v10, s[24:27], s42 offen offset:0 lds\n"
    "buffer_load_dwordx4 v11, s[24:27], s42 offen offset:1024 lds\n"
    "buffer_load_dwordx4 v12, s[24:27], s42 offen offset:2048 lds\n"
    "buffer_load_dwordx4 v13, s[24:27], s42 offen offset:3072 lds\n"
    "s_xor_b32 s57, s38, 1\n"
    "s_lshl_b32 s42, s57, 17\n"
    "s_add_u32 s42, s42, s54\n"
    "s_add_u32 s42, s42, 8192\n"
    "s_lshl_b32 s57, s57, 13\n"
    "s_add_u32 s57, s57, s56\n"
    "s_add_u32 m0, s57, 131072\n"
    "s_nop 0\n"
    "buffer_load_dwordx4 v18, s[16:19], s42 offen offset:0 lds\n"
    "buffer_load_dwordx4 v19, s[16:19], s42 offen offset:1024 lds\n"
    "buffer_load_dwordx4 v20, s[16:19], s42 offen offset:2048 lds\n"
    "buffer_load_dwordx4 v21, s[16:19], s42 offen offset:3072 lds\n"
    "s_xor_b32 s57, s38, 2\n"
    "s_lshl_b32 s42, s57, 17\n"
    "s_add_u32 s42, s42, s54\n"
    "s_add_u32 s42, s42, 8192\n"
    "s_lshl_b32 s57, s57, 13\n"
    "s_add_u32 s57, s57, s56\n"
    "s_add_u32 m0, s57, 131072\n"
    "s_nop 0\n"
    "buffer_load_dwordx4 v18, s[16:19], s42 offen offset:0 lds\n"
    "buffer_load_dwordx4 v19, s[16:19], s42 offen offset:1024 lds\n"
    "buffer_load_dwordx4 v20, s[16:19], s42 offen offset:2048 lds\n"
    "buffer_load_dwordx4 v21, s[16:19], s42 offen offset:3072 lds\n"
    "s_xor_b32 s57, s38, 3\n"
    "s_lshl_b32 s42, s57, 17\n"
    "s_add_u32 s42, s42, s54\n"
    "s_add_u32 s42, s42, 8192\n"
    "s_lshl_b32 s57, s57, 13\n"
    "s_add_u32 s57, s57, s56\n"
    "s_add_u32 m0, s57, 131072\n"
    "s_nop 0\n"
    "buffer_load_dwordx4 v18, s[16:19], s42 offen offset:0 lds\n"
    "buffer_load_dwordx4 v19, s[16:19], s42 offen offset:1024 lds\n"
    "buffer_load_dwordx4 v20, s[16:19], s42 offen offset:2048 lds\n"
    "buffer_load_dwordx4 v21, s[16:19], s42 offen offset:3072 lds\n"
    "s_xor_b32 s57, s39, 1\n"
    "s_mul_i32 s42, s57, 36864\n"
    "s_add_u32 s42, s42, s55\n"
    "s_add_u32 s42, s42, 294912\n"
    "s_lshl_b32 s57, s57, 13\n"
    "s_add_u32 s57, s57, s56\n"
    "s_add_u32 m0, s57, 65536\n"
    "s_nop 0\n"
    "buffer_load_dwordx4 v10, s[24:27], s42 offen offset:0 lds\n"
    "buffer_load_dwordx4 v11, s[24:27], s42 offen offset:1024 lds\n"
    "buffer_load_dwordx4 v12, s[24:27], s42 offen offset:2048 lds\n"
    "buffer_load_dwordx4 v13, s[24:27], s42 offen offset:3072 lds\n"
    "s_xor_b32 s57, s39, 2\n"
    "s_mul_i32 s42, s57, 36864\n"
    "s_add_u32 s42, s42, s55\n"
    "s_add_u32 s42, s42, 294912\n"
    "s_lshl_b32 s57, s57, 13\n"
    "s_add_u32 s57, s57, s56\n"
    "s_add_u32 m0, s57, 65536\n"
    "s_nop 0\n"
    "buffer_load_dwordx4 v10, s[24:27], s42 offen offset:0 lds\n"
    "buffer_load_dwordx4 v11, s[24:27], s42 offen offset:1024 lds\n"
    "buffer_load_dwordx4 v12, s[24:27], s42 offen offset:2048 lds\n"
    "buffer_load_dwordx4 v13, s[24:27], s42 offen offset:3072 lds\n"
    "s_xor_b32 s57, s39, 3\n"
    "s_mul_i32 s42, s57, 36864\n"
    "s_add_u32 s42, s42, s55\n"
    "s_add_u32 s42, s42, 294912\n"
    "s_lshl_b32 s57, s57, 13\n"
    "s_add_u32 s57, s57, s56\n"
    "s_add_u32 m0, s57, 65536\n"
    "s_nop 0\n"
    "buffer_load_dwordx4 v10, s[24:27], s42 offen offset:0 lds\n"
    "buffer_load_dwordx4 v11, s[24:27], s42 offen offset:1024 lds\n"
    "buffer_load_dwordx4 v12, s[24:27], s42 offen offset:2048 lds\n"
    "buffer_load_dwordx4 v13, s[24:27], s42 offen offset:3072 lds\n"
    "s_waitcnt vmcnt(24)\n"
    "s_barrier\n"
    "s_mul_i32 s42, s39, 36864\n"
    "s_add_u32 s42, s42, s55\n"
    "s_add_u32 s42, s42, 442368\n"
    "s_lshl_b32 s57, s39, 13\n"
    "s_add_u32 s57, s57, s56\n"
    "s_add_u32 m0, s57, 98304\n"
    "s_nop 0\n"
    "buffer_load_dwordx4 v10, s[24:27], s42 offen offset:0 lds\n"
    "buffer_load_dwordx4 v11, s[24:27], s42 offen offset:1024 lds\n"
    "buffer_load_dwordx4 v12, s[24:27], s42 offen offset:2048 lds\n"
    "buffer_load_dwordx4 v13, s[24:27], s42 offen offset:3072 lds\n"
    "s_xor_b32 s57, s39, 1\n"
    "s_mul_i32 s42, s57, 36864\n"
    "s_add_u32 s42, s42, s55\n"
    "s_add_u32 s42, s42, 442368\n"
    "s_lshl_b32 s57, s57, 13\n"
    "s_add_u32 s57, s57, s56\n"
    "s_add_u32 m0, s57, 98304\n"
    "s_nop 0\n"
    "buffer_load_dwordx4 v10, s[24:27], s42 offen offset:0 lds\n"
    "buffer_load_dwordx4 v11, s[24:27], s42 offen offset:1024 lds\n"
    "buffer_load_dwordx4 v12, s[24:27], s42 offen offset:2048 lds\n"
    "buffer_load_dwordx4 v13, s[24:27], s42 offen offset:3072 lds\n"
    "s_xor_b32 s57, s39, 2\n"
    "s_mul_i32 s42, s57, 36864\n"
    "s_add_u32 s42, s42, s55\n"
    "s_add_u32 s42, s42, 442368\n"
    "s_lshl_b32 s57, s57, 13\n"
    "s_add_u32 s57, s57, s56\n"
    "s_add_u32 m0, s57, 98304\n"
    "s_nop 0\n"
    "buffer_load_dwordx4 v10, s[24:27], s42 offen offset:0 lds\n"
    "buffer_load_dwordx4 v11, s[24:27], s42 offen offset:1024 lds\n"
    "buffer_load_dwordx4 v12, s[24:27], s42 offen offset:2048 lds\n"
    "buffer_load_dwordx4 v13, s[24:27], s42 offen offset:3072 lds\n"
    "s_xor_b32 s57, s39, 3\n"
    "s_mul_i32 s42, s57, 36864\n"
    "s_add_u32 s42, s42, s55\n"
    "s_add_u32 s42, s42, 442368\n"
    "s_lshl_b32 s57, s57, 13\n"
    "s_add_u32 s57, s57, s56\n"
    "s_add_u32 m0, s57, 98304\n"
    "s_nop 0\n"
    "buffer_load_dwordx4 v10, s[24:27], s42 offen offset:0 lds\n"
    "buffer_load_dwordx4 v11, s[24:27], s42 offen offset:1024 lds\n"
    "buffer_load_dwordx4 v12, s[24:27], s42 offen offset:2048 lds\n"
    "buffer_load_dwordx4 v13, s[24:27], s42 offen offset:3072 lds\n"
    "s_waitcnt vmcnt(16)\n"
    "s_barrier\n"
    ".Lr2pre:\n"
    "s_mov_b32 s42, 1\n"
    "s_branch .Lmainred\n"
    ".Lr2p:\n"
    ".Lpad_2b_0:\n"
    "s_cbranch_execz .Lpad_2b_1\n"
    "v_lshlrev_b32 v2, 4, v0\n"
    "v_lshlrev_b32 v3, 3, v0\n"
    "s_lshl2_add_u32 s42, s37, s39\n"
    "s_lshl_b32 s50, s42, 15\n"
    "s_add_u32 s50, s50, 262144\n"
    "s_and_b32 s42, s41, 2\n"
    "s_add_u32 s42, s42, s41\n"
    "s_lshl_b32 s42, s42, 12\n"
    "s_add_u32 s50, s50, s42\n"
    "s_add_u32 s51, s50, 8192\n"
    "v_add_u32 v8, s42, v2\n"
    "s_lshl_b32 s53, s2, 6\n"
    "s_add_u32 s53, s53, 16384\n"
    "s_waitcnt lgkmcnt(0)\n"
    "s_add_u32 s96, s96, 0x7f4a7c15\n"
    "s_addc_u32 s97, s97, 0x9e3779b9\n"
    "v_mov_b32 v14, 0x20000\n"
    "v_mov_b32 v15, 0\n"
    "s_waitcnt vmcnt(0)\n"
    "v_mov_b64 v[22:23], 0\n"
    "s_mov_b64 exec, 15\n"
    "buffer_atomic_add_x2 v[22:23], v3, s[28:31], s53 offen sc0 sc1\n"
    "s_barrier\n"
    "s_waitcnt vmcnt(0)\n"
    "v_cmp_ne_u64 vcc, s[96:97], v[22:23]\n"
    "s_mov_b64 exec, -1\n"
    "s_cbranch_vccz .Lr2p_go\n"
    "s_mov_b32 s45, 524288\n"
    "s_mov_b64 exec, 15\n"
    ".Lpoll1:\n"
    "v_mov_b64 v[22:23], 0\n"
    "buffer_atomic_add_x2 v[22:23], v3, s[28:31], s53 offen sc0 sc1\n"
    "s_sub_u32 s45, s45, 1\n"
    "s_cbranch_scc1 .Lpolld1\n"
    "s_waitcnt vmcnt(0)\n"
    "v_cmp_ne_u64 vcc, s[96:97], v[22:23]\n"
    "s_nop 1\n"
    "s_cbranch_vccnz .Lpoll1\n"
    ".Lpolld1:\n"
    "s_mov_b64 exec, -1\n"
    ".Lr2p_go:\n"
    "buffer_load_dwordx4 v[32:35], v2, s[28:31], s50 offen offset:0 sc1\n"
    "buffer_load_dwordx4 v[36:39], v2, s[28:31], s50 offen offset:1024 sc1\n"
    "buffer_load_dwordx4 v[40:43], v2, s[28:31], s50 offen offset:2048 sc1\n"
    "buffer_load_dwordx4 v[44:47], v2, s[28:31], s50 offen offset:3072 sc1\n"
    "s_sleep 3\n"
    "buffer_load_dwordx4 v[48:51], v2, s[28:31], s51 offen offset:0 sc1\n"
    "buffer_load_dwordx4 v[52:55], v2, s[28:31], s51 offen offset:1024 sc1\n"
    "buffer_load_dwordx4 v[56:59], v2, s[28:31], s51 offen offset:2048 sc1\n"
    "buffer_load_dwordx4 v[60:63], v2, s[28:31], s51 offen offset:3072 sc1\n"
    "s_waitcnt vmcnt(7)\n"
    "ds_write_b128 v8, v[32:35] offset:0\n"
    "v_or3_b32 v1, v32, v33, v34\n"
    "v_or_b32 v1, v1, v35\n"
    "s_waitcnt vmcnt(6)\n"
    "ds_write_b128 v8, v[36:39] offset:1024\n"
    "v_or3_b32 v1, v1, v36, v37\n"
    "v_or3_b32 v1, v1, v38, v39\n"
    "s_waitcnt vmcnt(5)\n"
    "ds_write_b128 v8, v[40:43] offset:2048\n"
    "v_or3_b32 v1, v1, v40, v41\n"
    "v_or3_b32 v1, v1, v42, v43\n"
    "s_waitcnt vmcnt(4)\n"
    "ds_write_b128 v8, v[44:47] offset:3072\n"
    "v_or3_b32 v1, v1, v44, v45\n"
    "v_or3_b32 v1, v1, v46, v47\n"
    "v_cmp_gt_i32 vcc, 0, v1\n"
    "s_waitcnt lgkmcnt(0)\n"
    "s_cbranch_vccz .Lr2p_a\n"
    "buffer_load_dwordx4 v[32:35], v2, s[28:31], s50 offen offset:0 sc1\n"
    "buffer_load_dwordx4 v[36:39], v2, s[28:31], s50 offen offset:1024 sc1\n"
    "buffer_load_dwordx4 v[40:43], v2, s[28:31], s50 offen offset:2048 sc1\n"
    "buffer_load_dwordx4 v[44:47], v2, s[28:31], s50 offen offset:3072 sc1\n"
    "s_mov_b32 s45, 524288\n"
    ".Lpl2:\n"
    "s_waitcnt vmcnt(3)\n"
    "ds_write_b128 v8, v[32:35] offset:0\n"
    "v_or3_b32 v1, v32, v33, v34\n"
    "v_or_b32 v1, v1, v35\n"
    "s_waitcnt vmcnt(2)\n"
    "ds_write_b128 v8, v[36:39] offset:1024\n"
    "v_or3_b32 v1, v1, v36, v37\n"
    "v_or3_b32 v1, v1, v38, v39\n"
    "s_waitcnt vmcnt(1)\n"
    "ds_write_b128 v8, v[40:43] offset:2048\n"
    "v_or3_b32 v1, v1, v40, v41\n"
    "v_or3_b32 v1, v1, v42, v43\n"
    "s_waitcnt vmcnt(0)\n"
    "ds_write_b128 v8, v[44:47] offset:3072\n"
    "v_or3_b32 v1, v1, v44, v45\n"
    "v_or3_b32 v1, v1, v46, v47\n"
    "v_cmp_gt_i32 vcc, 0, v1\n"
    "s_sub_u32 s45, s45, 1\n"
    "s_cbranch_scc1 .Lpld2\n"
    "s_cbranch_vccz .Lpld2\n"
    "s_waitcnt lgkmcnt(0)\n"
    "buffer_load_dwordx4 v[32:35], v2, s[28:31], s50 offen offset:0 sc1\n"
    "buffer_load_dwordx4 v[36:39], v2, s[28:31], s50 offen offset:1024 sc1\n"
    "buffer_load_dwordx4 v[40:43], v2, s[28:31], s50 offen offset:2048 sc1\n"
    "buffer_load_dwordx4 v[44:47], v2, s[28:31], s50 offen offset:3072 sc1\n"
    "s_branch .Lpl2\n"
    ".Lpld2:\n"
    "s_waitcnt lgkmcnt(0)\n"
    ".Lr2p_a:\n"
    "s_barrier\n"
    "s_mov_b32 s45, 524288\n"
    ".Lpl3:\n"
    "s_waitcnt vmcnt(3)\n"
    "ds_write_b128 v8, v[48:51] offset:8192\n"
    "v_or3_b32 v1, v48, v49, v50\n"
    "v_or_b32 v1, v1, v51\n"
    "s_waitcnt vmcnt(2)\n"
    "ds_write_b128 v8, v[52:55] offset:9216\n"
    "v_or3_b32 v1, v1, v52, v53\n"
    "v_or3_b32 v1, v1, v54, v55\n"
    "s_waitcnt vmcnt(1)\n"
    "ds_write_b128 v8, v[56:59] offset:10240\n"
    "v_or3_b32 v1, v1, v56, v57\n"
    "v_or3_b32 v1, v1, v58, v59\n"
    "s_waitcnt vmcnt(0)\n"
    "ds_write_b128 v8, v[60:63] offset:11264\n"
    "v_or3_b32 v1, v1, v60, v61\n"
    "v_or3_b32 v1, v1, v62, v63\n"
    "v_cmp_gt_i32 vcc, 0, v1\n"
    "s_sub_u32 s45, s45, 1\n"
    "s_cbranch_scc1 .Lpld3\n"
    "s_cbranch_vccz .Lpld3\n"
    "s_waitcnt lgkmcnt(0)\n"
    "buffer_load_dwordx4 v[48:51], v2, s[28:31], s51 offen offset:0 sc1\n"
    "buffer_load_dwordx4 v[52:55], v2, s[28:31], s51 offen offset:1024 sc1\n"
    "buffer_load_dwordx4 v[56:59], v2, s[28:31], s51 offen offset:2048 sc1\n"
    "buffer_load_dwordx4 v[60:63], v2, s[28:31], s51 offen offset:3072 sc1\n"
    "s_branch .Lpl3\n"
    ".Lpld3:\n"
    "s_waitcnt lgkmcnt(0)\n"
    "s_barrier\n"
    "ds_write_b32 v14, v15\n"
    "s_waitcnt lgkmcnt(0)\n"
    "s_barrier\n"
    "s_branch .Lred_nobar\n"
    ".Lr2h:\n"
    "s_lshl_b32 s46, s2, 6\n"
    "s_waitcnt lgkmcnt(0)\n"
    "s_buffer_load_dwordx2 s[96:97], s[28:31], s46 offset:24\n"
    "s_cmp_lg_u32 s41, 2\n"
    "s_cbranch_scc1 .Lr2h_nt\n"
    "s_sleep 30\n"
    "s_add_u32 s47, s46, 15872\n"
    "s_lshl1_add_u32 s48, s46, 162816\n"
    "v_mov_b64 v[22:23], 0\n"
    "s_mov_b64 exec, 1\n"
    "buffer_atomic_add_x2 v[22:23], off, s[28:31], s47 sc1\n"
    "buffer_atomic_add_x2 v[22:23], off, s[28:31], s47 offset:512 sc1\n"
    "buffer_atomic_add_x2 v[22:23], off, s[28:31], s48 sc1\n"
    "buffer_atomic_add_x2 v[22:23], off, s[28:31], s48 offset:64 sc1\n"
    "buffer_atomic_add_x2 v[22:23], off, s[28:31], s48 offset:1088 sc1\n"
    "s_mov_b64 exec, -1\n"
    ".Lr2h_nt:\n"
    "s_barrier\n"
    "s_barrier\n"
    "s_barrier\n"
    "s_branch .Lr2pre\n"
    ".Lr1l_setup:\n"
    "s_lshl_b32 s56, s2, 6\n"
    "s_buffer_load_dwordx2 s[96:97], s[28:31], s56 offset:16\n"
    "s_buffer_load_dwordx2 s[98:99], s[28:31], s56 offset:24\n"
    "s_lshl3_add_u32 s44, s43, s41\n"
    "s_lshl_b32 s44, s44, 7\n"
    "s_buffer_load_dwordx2 s[58:59], s[28:31], s44 offset:16\n"
    "s_buffer_load_dwordx2 s[4:5], s[28:31], s44 offset:80\n"
    "s_lshl2_add_u32 s57, s37, s44\n"
    "s_lshl1_add_u32 s57, s57, 163840\n"
    "s_lshl4_add_u32 s44, s53, s37\n"
    "s_lshl_b32 s44, s44, 6\n"
    "s_buffer_load_dwordx2 s[6:7], s[28:31], s44 offset:24\n"
    "s_buffer_load_dwordx2 s[0:1], s[28:31], s44 offset:536\n"
    "s_lshl3_add_u32 s3, s38, s44\n"
    "s_add_u32 s3, s3, 16384\n"
    "s_add_u32 s40, s56, 16384\n"
    "v_mov_b64 v[32:33], -1\n"
    "v_mov_b64 v[34:35], -1\n"
    "v_xor_b32 v9, v22, v23\n"
    "v_lshlrev_b32 v9, 4, v9\n"
    "v_lshl_or_b32 v18, v22, 8, v9\n"
    "v_lshl_or_b32 v9, v23, 8, v9\n"
    "v_xor_b32 v19, 64, v18\n"
    "v_xor_b32 v20, 0x80, v18\n"
    "v_xor_b32 v21, 0xc0, v18\n"
    "v_lshlrev_b32 v2, 4, v0\n"
    "v_lshlrev_b32 v3, 3, v0\n"
    "s_lshl_b32 s42, s53, 4\n"
    "s_lshl1_add_u32 s42, s37, s42\n"
    "s_lshr_b32 s50, s38, 1\n"
    "s_add_u32 s42, s42, s50\n"
    "s_lshl_b32 s54, s42, 13\n"
    "s_lshl_b32 s55, s41, 13\n"
    "s_add_u32 s55, s55, 0x20000\n"
    "s_and_b32 s42, s38, 1\n"
    "s_lshl_b32 s42, s42, 12\n"
    "s_add_u32 s54, s54, s42\n"
    "s_add_u32 s55, s55, s42\n"
    "s_branch .Lr1l_loop\n"
    ".Lr1l_tail:\n"
    ".Lpad_1a_0:\n"
    "s_cbranch_execz .Lpad_1a_1\n"
    "s_xor_b32 s46, s54, 4096\n"
    "s_xor_b32 s47, s55, 4096\n"
    "s_lshl4_add_u32 s42, s37, s53\n"
    "s_lshl2_add_u32 s42, s42, s38\n"
    "s_lshl_b32 s52, s42, 11\n"
    "s_add_u32 s52, s52, 262144\n"
    "s_mov_b32 m0, s55\n"
    "s_nop 0\n"
    "buffer_load_dwordx4 v18, s[12:15], s54 offen offset:0 lds\n"
    "buffer_load_dwordx4 v19, s[12:15], s54 offen offset:1024 lds\n"
    "buffer_load_dwordx4 v20, s[12:15], s54 offen offset:2048 lds\n"
    "buffer_load_dwordx4 v21, s[12:15], s54 offen offset:3072 lds\n"
    "buffer_store_dwordx4 v[32:35], v2, s[28:31], s52 offen offset:0 sc1\n"
    "buffer_store_dwordx4 v[32:35], v2, s[28:31], s52 offen offset:1024 sc1\n"
    "s_waitcnt vmcnt(14)\n"
    "s_barrier\n"
    "s_mov_b32 m0, s47\n"
    "s_nop 0\n"
    "buffer_load_dwordx4 v18, s[12:15], s46 offen offset:0 lds\n"
    "buffer_load_dwordx4 v19, s[12:15], s46 offen offset:1024 lds\n"
    "buffer_load_dwordx4 v20, s[12:15], s46 offen offset:2048 lds\n"
    "buffer_load_dwordx4 v21, s[12:15], s46 offen offset:3072 lds\n"
    "s_lshl2_add_u32 s42, s37, s41\n"
    "s_mul_i32 s46, s42, 147456\n"
    "s_add_u32 s46, s46, 1024\n"
    "s_lshl_b32 s50, s41, 14\n"
    "s_add_u32 s50, s50, 0x8000\n"
    "s_mov_b32 s49, s50\n"
    "s_lshl_b32 s42, s43, 4\n"
    "s_lshl1_add_u32 s42, s37, s42\n"
    "s_lshl_b32 s47, s42, 13\n"
    "s_add_u32 s47, s47, s48\n"
    "s_add_u32 s51, s48, 0x18000\n"
    "s_waitcnt lgkmcnt(0)\n"
    "s_add_u32 s96, s96, 0x7f4a7c15\n"
    "s_addc_u32 s97, s97, 0x9e3779b9\n"
    "s_add_u32 s98, s98, 0x7f4a7c15\n"
    "s_addc_u32 s99, s99, 0x9e3779b9\n"
    "s_add_u32 s58, s58, 0x7f4a7c15\n"
    "s_addc_u32 s59, s59, 0x9e3779b9\n"
    "s_add_u32 s4, s4, 0x7f4a7c15\n"
    "s_addc_u32 s5, s5, 0x9e3779b9\n"
    "s_add_u32 s6, s6, 0x7f4a7c15\n"
    "s_addc_u32 s7, s7, 0x9e3779b9\n"
    "s_add_u32 s0, s0, 0x7f4a7c15\n"
    "s_addc_u32 s1, s1, 0x9e3779b9\n"
    "v_mov_b64 v[36:37], s[6:7]\n"
    "v_mov_b64 v[38:39], s[0:1]\n"
    "v_mov_b64 v[4:5], s[58:59]\n"
    "v_mov_b64 v[6:7], s[4:5]\n"
    "s_waitcnt vmcnt(10)\n"
    "s_barrier\n"
    "s_mov_b32 m0, s49\n"
    "s_nop 0\n"
    "buffer_load_dwordx4 v14, s[24:27], s46 offen offset:0 lds\n"
    "buffer_load_dwordx4 v15, s[24:27], s46 offen offset:1024 lds\n"
    "buffer_load_dwordx4 v16, s[24:27], s46 offen offset:2048 lds\n"
    "buffer_load_dwordx4 v17, s[24:27], s46 offen offset:3072 lds\n"
    "s_add_u32 s46, s46, 36864\n"
    "s_add_u32 s49, s49, 4096\n"
    "s_mov_b32 m0, s49\n"
    "s_nop 0\n"
    "buffer_load_dwordx4 v14, s[24:27], s46 offen offset:0 lds\n"
    "buffer_load_dwordx4 v15, s[24:27], s46 offen offset:1024 lds\n"
    "buffer_load_dwordx4 v16, s[24:27], s46 offen offset:2048 lds\n"
    "buffer_load_dwordx4 v17, s[24:27], s46 offen offset:3072 lds\n"
    "s_add_u32 s46, s46, 36864\n"
    "s_add_u32 s49, s49, 4096\n"
    "s_waitcnt vmcnt(8)\n"
    "s_barrier\n"
    "s_mov_b32 m0, s51\n"
    "s_nop 0\n"
    "buffer_load_dwordx4 v18, s[16:19], s47 offen offset:0 lds\n"
    "buffer_load_dwordx4 v19, s[16:19], s47 offen offset:1024 lds\n"
    "buffer_load_dwordx4 v20, s[16:19], s47 offen offset:2048 lds\n"
    "buffer_load_dwordx4 v21, s[16:19], s47 offen offset:3072 lds\n"
    "v_bfe_u32 v23, v0, 2, 2\n"
    "v_lshl_add_u32 v24, v22, 3, v23\n"
    "v_lshlrev_b32 v24, 7, v24\n"
    "v_lshrrev_b32 v25, 1, v0\n"
    "v_lshrrev_b32 v26, 3, v0\n"
    "v_xor_b32 v25, v25, v26\n"
    "v_and_b32 v25, 1, v25\n"
    "v_lshl_or_b32 v24, v25, 4, v24\n"
    "v_and_or_b32 v24, v3, 8, v24\n"
    "v_lshlrev_b32 v25, 2, v0\n"
    "v_and_or_b32 v24, v25, 64, v24\n"
    "v_add_u32 v10, s50, v24\n"
    "v_xor_b32 v11, 32, v10\n"
    "v_xor_b32 v12, 64, v10\n"
    "v_xor_b32 v13, 0x60, v10\n"
    "v_add_u32 v8, s50, v2\n"
    "s_lshr_b32 s47, s41, 1\n"
    "s_lshl_b32 s42, s47, 13\n"
    "s_add_u32 s42, s42, 0x18000\n"
    "v_add_u32 v9, s42, v9\n"
    "s_waitcnt vmcnt(4)\n"
    "s_mov_b32 m0, s49\n"
    "s_nop 0\n"
    "buffer_load_dwordx4 v14, s[24:27], s46 offen offset:0 lds\n"
    "buffer_load_dwordx4 v15, s[24:27], s46 offen offset:1024 lds\n"
    "buffer_load_dwordx4 v16, s[24:27], s46 offen offset:2048 lds\n"
    "buffer_load_dwordx4 v17, s[24:27], s46 offen offset:3072 lds\n"
    "s_add_u32 s46, s46, 36864\n"
    "s_add_u32 s49, s49, 4096\n"
    "s_sleep 1\n"
    "s_mov_b64 exec, 1\n"
    "buffer_atomic_swap_x2 v[38:39], off, s[28:31], s3 offset:512 sc1\n"
    "buffer_atomic_swap_x2 v[36:37], off, s[28:31], s3 sc1\n"
    "s_mov_b64 exec, -1\n"
    "s_waitcnt vmcnt(6)\n"
    "s_mov_b32 m0, s49\n"
    "s_nop 0\n"
    "buffer_load_dwordx4 v14, s[24:27], s46 offen offset:0 lds\n"
    "buffer_load_dwordx4 v15, s[24:27], s46 offen offset:1024 lds\n"
    "buffer_load_dwordx4 v16, s[24:27], s46 offen offset:2048 lds\n"
    "buffer_load_dwordx4 v17, s[24:27], s46 offen offset:3072 lds\n"
    "s_lshl4_add_u32 s42, s37, s43\n"
    "s_lshl2_add_u32 s42, s42, s41\n"
    "s_lshl_b32 s46, s42, 11\n"
    "s_lshl1_add_u32 s42, s43, s47\n"
    "s_lshl3_add_u32 s42, s42, s37\n"
    "s_lshl_b32 s48, s42, 11\n"
    "s_and_b32 s42, s41, 1\n"
    "s_lshl_b32 s42, s42, 10\n"
    "s_add_u32 s48, s48, s42\n"
    "s_lshl_b32 s42, s41, 11\n"
    "s_add_u32 s42, s42, 0x8000\n"
    "v_add_u32 v31, s42, v2\n"
    "s_add_u32 s46, s46, 262144\n"
    "s_add_u32 s48, s48, 1310720\n"
    "v_mov_b64 v[28:29], 0\n"
    "s_barrier\n"
    "ds_read_b128 v[64:67], v9 offset:0\n"
    "ds_read_b128 v[68:71], v9 offset:4096\n"
    "v_xor_b32 v23, 64, v9\n"
    "ds_read_b128 v[72:75], v23 offset:0\n"
    "ds_read_b128 v[76:79], v23 offset:4096\n"
    "v_xor_b32 v22, 128, v9\n"
    "ds_read_b128 v[80:83], v22 offset:0\n"
    "ds_read_b128 v[84:87], v22 offset:4096\n"
    "v_xor_b32 v23, 192, v9\n"
    "ds_read_b128 v[88:91], v23 offset:0\n"
    "ds_read_b128 v[92:95], v23 offset:4096\n"
    "s_waitcnt vmcnt(0)\n"
    "s_waitcnt lgkmcnt(4)\n"
    "ds_read_b64_tr_b16 v[96:97], v10 offset:0\n"
    "ds_read_b64_tr_b16 v[98:99], v11 offset:512\n"
    "ds_read_b64_tr_b16 v[100:101], v10 offset:4096\n"
    "ds_read_b64_tr_b16 v[102:103], v11 offset:4608\n"
    "ds_read_b64_tr_b16 v[104:105], v10 offset:8192\n"
    "ds_read_b64_tr_b16 v[106:107], v11 offset:8704\n"
    "ds_read_b64_tr_b16 v[108:109], v10 offset:12288\n"
    "ds_read_b64_tr_b16 v[110:111], v11 offset:12800\n"
    "ds_read_b64_tr_b16 v[112:113], v11 offset:0\n"
    "ds_read_b64_tr_b16 v[114:115], v10 offset:512\n"
    "s_waitcnt lgkmcnt(8)\n"
    "v_mfma_f32_16x16x32_bf16 v[128:131], v[64:67], v[96:99], 0\n"
    "v_mfma_f32_16x16x32_bf16 v[132:135], v[68:71], v[96:99], 0\n"
    "ds_read_b64_tr_b16 v[116:117], v11 offset:4096\n"
    "ds_read_b64_tr_b16 v[118:119], v10 offset:4608\n"
    "s_waitcnt lgkmcnt(8)\n"
    "v_mfma_f32_16x16x32_bf16 v[128:131], v[72:75], v[100:103], v[128:131]\n"
    "v_mfma_f32_16x16x32_bf16 v[132:135], v[76:79], v[100:103], v[132:135]\n"
    "ds_read_b64_tr_b16 v[120:121], v11 offset:8192\n"
    "ds_read_b64_tr_b16 v[122:123], v10 offset:8704\n"
    "s_waitcnt lgkmcnt(8)\n"
    "v_mfma_f32_16x16x32_bf16 v[128:131], v[80:83], v[104:107], v[128:131]\n"
    "v_mfma_f32_16x16x32_bf16 v[132:135], v[84:87], v[104:107], v[132:135]\n"
    "ds_read_b64_tr_b16 v[124:125], v11 offset:12288\n"
    "ds_read_b64_tr_b16 v[126:127], v10 offset:12800\n"
    "s_mov_b64 exec, 15\n"
    "buffer_atomic_add_x2 v[28:29], v3, s[28:31], s40 offen sc0 sc1\n"
    "s_mov_b64 exec, -1\n"
    "s_waitcnt lgkmcnt(8)\n"
    "v_mfma_f32_16x16x32_bf16 v[128:131], v[88:91], v[108:111], v[128:131]\n"
    "v_mfma_f32_16x16x32_bf16 v[132:135], v[92:95], v[108:111], v[132:135]\n"
    "ds_read_b64_tr_b16 v[96:97], v12 offset:0\n"
    "ds_read_b64_tr_b16 v[98:99], v13 offset:512\n"
    "s_waitcnt lgkmcnt(8)\n"
    "v_mfma_f32_16x16x32_bf16 v[136:139], v[64:67], v[112:115], 0\n"
    "v_mfma_f32_16x16x32_bf16 v[140:143], v[68:71], v[112:115], 0\n"
    "ds_read_b64_tr_b16 v[100:101], v12 offset:4096\n"
    "ds_read_b64_tr_b16 v[102:103], v13 offset:4608\n"
    "s_waitcnt lgkmcnt(8)\n"
    "v_mfma_f32_16x16x32_bf16 v[136:139], v[72:75], v[116:119], v[136:139]\n"
    "v_mfma_f32_16x16x32_bf16 v[140:143], v[76:79], v[116:119], v[140:143]\n"
    "ds_read_b64_tr_b16 v[104:105], v12 offset:8192\n"
    "ds_read_b64_tr_b16 v[106:107], v13 offset:8704\n"
    "s_waitcnt lgkmcnt(8)\n"
    "v_mfma_f32_16x16x32_bf16 v[136:139], v[80:83], v[120:123], v[136:139]\n"
    "v_mfma_f32_16x16x32_bf16 v[140:143], v[84:87], v[120:123], v[140:143]\n"
    "ds_read_b64_tr_b16 v[108:109], v12 offset:12288\n"
    "ds_read_b64_tr_b16 v[110:111], v13 offset:12800\n"
    "v_cvt_pk_bf16_f32 v160, v128, v129\n"
    "v_cvt_pk_bf16_f32 v161, v130, v131\n"
    "s_waitcnt lgkmcnt(8)\n"
    "v_mfma_f32_16x16x32_bf16 v[136:139], v[88:91], v[124:127], v[136:139]\n"
    "v_mfma_f32_16x16x32_bf16 v[140:143], v[92:95], v[124:127], v[140:143]\n"
    "ds_read_b64_tr_b16 v[112:113], v13 offset:0\n"
    "ds_read_b64_tr_b16 v[114:115], v12 offset:512\n"
    "v_cvt_pk_bf16_f32 v162, v132, v133\n"
    "v_cvt_pk_bf16_f32 v163, v134, v135\n"
    "s_waitcnt lgkmcnt(8)\n"
    "v_mfma_f32_16x16x32_bf16 v[144:147], v[64:67], v[96:99], 0\n"
    "v_mfma_f32_16x16x32_bf16 v[148:151], v[68:71], v[96:99], 0\n"
    "ds_read_b64_tr_b16 v[116:117], v13 offset:4096\n"
    "ds_read_b64_tr_b16 v[118:119], v12 offset:4608\n"
    "s_waitcnt lgkmcnt(8)\n"
    "v_mfma_f32_16x16x32_bf16 v[144:147], v[72:75], v[100:103], v[144:147]\n"
    "v_mfma_f32_16x16x32_bf16 v[148:151], v[76:79], v[100:103], v[148:151]\n"
    "ds_read_b64_tr_b16 v[120:121], v13 offset:8192\n"
    "ds_read_b64_tr_b16 v[122:123], v12 offset:8704\n"
    "s_waitcnt lgkmcnt(8)\n"
    "v_mfma_f32_16x16x32_bf16 v[144:147], v[80:83], v[104:107], v[144:147]\n"
    "v_mfma_f32_16x16x32_bf16 v[148:151], v[84:87], v[104:107], v[148:151]\n"
    "ds_read_b64_tr_b16 v[124:125], v13 offset:12288\n"
    "ds_read_b64_tr_b16 v[126:127], v12 offset:12800\n"
    "v_cvt_pk_bf16_f32 v164, v136, v137\n"
    "v_cvt_pk_bf16_f32 v165, v138, v139\n"
    "s_waitcnt lgkmcnt(8)\n"
    "v_mfma_f32_16x16x32_bf16 v[144:147], v[88:91], v[108:111], v[144:147]\n"
    "v_mfma_f32_16x16x32_bf16 v[148:151], v[92:95], v[108:111], v[148:151]\n"
    "v_cvt_pk_bf16_f32 v166, v140, v141\n"
    "v_cvt_pk_bf16_f32 v167, v142, v143\n"
    "s_waitcnt lgkmcnt(6)\n"
    "v_mfma_f32_16x16x32_bf16 v[152:155], v[64:67], v[112:115], 0\n"
    "v_mfma_f32_16x16x32_bf16 v[156:159], v[68:71], v[112:115], 0\n"
    "s_waitcnt lgkmcnt(4)\n"
    "v_mfma_f32_16x16x32_bf16 v[152:155], v[72:75], v[116:119], v[152:155]\n"
    "v_mfma_f32_16x16x32_bf16 v[156:159], v[76:79], v[116:119], v[156:159]\n"
    "s_waitcnt lgkmcnt(2)\n"
    "v_mfma_f32_16x16x32_bf16 v[152:155], v[80:83], v[120:123], v[152:155]\n"
    "v_mfma_f32_16x16x32_bf16 v[156:159], v[84:87], v[120:123], v[156:159]\n"
    "v_cvt_pk_bf16_f32 v168, v144, v145\n"
    "v_cvt_pk_bf16_f32 v169, v146, v147\n"
    "s_waitcnt lgkmcnt(0)\n"
    "v_mfma_f32_16x16x32_bf16 v[152:155], v[88:91], v[124:127], v[152:155]\n"
    "v_mfma_f32_16x16x32_bf16 v[156:159], v[92:95], v[124:127], v[156:159]\n"
    "v_cvt_pk_bf16_f32 v170, v148, v149\n"
    "v_cvt_pk_bf16_f32 v171, v150, v151\n"
    "s_waitcnt vmcnt(0)\n"
    "s_mov_b64 exec, 15\n"
    "v_cmp_ne_u64 vcc, s[98:99], v[28:29]\n"
    "s_mov_b64 exec, -1\n"
    "s_cbranch_vccz .Lr1l_ready\n"
    "s_mov_b32 s45, 524288\n"
    "s_mov_b64 exec, 15\n"
    ".Lpoll4:\n"
    "v_mov_b64 v[28:29], 0\n"
    "buffer_atomic_add_x2 v[28:29], v3, s[28:31], s40 offen sc0 sc1\n"
    "s_sub_u32 s45, s45, 1\n"
    "s_cbranch_scc1 .Lpolld4\n"
    "s_waitcnt vmcnt(0)\n"
    "v_cmp_ne_u64 vcc, s[98:99], v[28:29]\n"
    "s_nop 1\n"
    "s_cbranch_vccnz .Lpoll4\n"
    ".Lpolld4:\n"
    "s_mov_b64 exec, -1\n"
    ".Lr1l_ready:\n"
    ".Lpad_1b_0:\n"
    "s_cbranch_execz .Lpadret_1b\n"
    "buffer_load_dwordx4 v[192:195], v2, s[28:31], s46 offen offset:0 sc1\n"
    "buffer_load_dwordx4 v[196:199], v2, s[28:31], s46 offen offset:1024 sc1\n"
    "v_cvt_pk_bf16_f32 v172, v152, v153\n"
    "v_cvt_pk_bf16_f32 v173, v154, v155\n"
    "v_cvt_pk_bf16_f32 v174, v156, v157\n"
    "v_cvt_pk_bf16_f32 v175, v158, v159\n"
    "s_mov_b32 s45, 524288\n"
    ".Lpl5:\n"
    "s_waitcnt vmcnt(1)\n"
    "v_or3_b32 v1, v192, v193, v194\n"
    "v_or_b32 v1, v1, v195\n"
    "s_waitcnt vmcnt(0)\n"
    "v_or3_b32 v1, v1, v196, v197\n"
    "v_or3_b32 v1, v1, v198, v199\n"
    "v_cmp_gt_i32 vcc, 0, v1\n"
    "s_sub_u32 s45, s45, 1\n"
    "s_cbranch_scc1 .Lpld5\n"
    "s_cbranch_vccz .Lpld5\n"
    "buffer_load_dwordx4 v[192:195], v2, s[28:31], s46 offen offset:0 sc1\n"
    "buffer_load_dwordx4 v[196:199], v2, s[28:31], s46 offen offset:1024 sc1\n"
    "s_branch .Lpl5\n"
    ".Lpld5:\n"
    "v_mfma_f32_16x16x32_bf16 v[128:131], v[160:163], v[192:195], 0\n"
    "v_mfma_f32_16x16x32_bf16 v[132:135], v[164:167], v[192:195], 0\n"
    "v_mfma_f32_16x16x32_bf16 v[136:139], v[160:163], v[196:199], 0\n"
    "v_mfma_f32_16x16x32_bf16 v[140:143], v[164:167], v[196:199], 0\n"
    "v_mfma_f32_16x16x32_bf16 v[144:147], v[168:171], v[192:195], 0\n"
    "v_mfma_f32_16x16x32_bf16 v[148:151], v[172:175], v[192:195], 0\n"
    "v_mfma_f32_16x16x32_bf16 v[152:155], v[168:171], v[196:199], 0\n"
    "v_mfma_f32_16x16x32_bf16 v[156:159], v[172:175], v[196:199], 0\n"
    "s_nop 0\n"
    "ds_write_b128 v8, v[128:131] offset:0\n"
    "ds_write_b128 v8, v[132:135] offset:1024\n"
    "ds_write_b128 v8, v[136:139] offset:2048\n"
    "ds_write_b128 v8, v[140:143] offset:3072\n"
    "ds_write_b128 v8, v[144:147] offset:4096\n"
    "ds_write_b128 v8, v[148:151] offset:5120\n"
    "ds_write_b128 v8, v[152:155] offset:6144\n"
    "ds_write_b128 v8, v[156:159] offset:7168\n"
    "s_waitcnt lgkmcnt(0)\n"
    "s_barrier\n"
    "ds_read_b128 v[64:67], v31 offset:0\n"
    "ds_read_b128 v[68:71], v31 offset:1024\n"
    "ds_read_b128 v[72:75], v31 offset:16384\n"
    "ds_read_b128 v[76:79], v31 offset:17408\n"
    "ds_read_b128 v[80:83], v31 offset:32768\n"
    "ds_read_b128 v[84:87], v31 offset:33792\n"
    "ds_read_b128 v[88:91], v31 offset:49152\n"
    "ds_read_b128 v[92:95], v31 offset:50176\n"
    "s_waitcnt lgkmcnt(4)\n"
    "v_pk_add_f32 v[64:65], v[64:65], v[72:73]\n"
    "v_pk_add_f32 v[66:67], v[66:67], v[74:75]\n"
    "v_pk_add_f32 v[68:69], v[68:69], v[76:77]\n"
    "v_pk_add_f32 v[70:71], v[70:71], v[78:79]\n"
    "s_waitcnt lgkmcnt(2)\n"
    "v_pk_add_f32 v[64:65], v[64:65], v[80:81]\n"
    "v_pk_add_f32 v[66:67], v[66:67], v[82:83]\n"
    "v_pk_add_f32 v[68:69], v[68:69], v[84:85]\n"
    "v_pk_add_f32 v[70:71], v[70:71], v[86:87]\n"
    "s_waitcnt lgkmcnt(0)\n"
    "v_pk_add_f32 v[64:65], v[64:65], v[88:89]\n"
    "v_pk_add_f32 v[66:67], v[66:67], v[90:91]\n"
    "v_pk_add_f32 v[68:69], v[68:69], v[92:93]\n"
    "v_pk_add_f32 v[70:71], v[70:71], v[94:95]\n"
    "v_cvt_pk_bf16_f32 v96, v64, v65\n"
    "v_cvt_pk_bf16_f32 v97, v66, v67\n"
    "v_cvt_pk_bf16_f32 v98, v68, v69\n"
    "v_cvt_pk_bf16_f32 v99, v70, v71\n"
    "buffer_store_dwordx4 v[96:99], v2, s[28:31], s48 offen sc1\n"
    "s_mov_b64 exec, 1\n"
    "s_waitcnt vmcnt(0)\n"
    "buffer_atomic_swap_x2 v[4:5], off, s[28:31], s57 sc1\n"
    "buffer_atomic_swap_x2 v[6:7], off, s[28:31], s57 offset:128 sc1\n"
    "s_mov_b64 exec, -1\n"
    "s_lshl1_add_u32 s40, s56, 163904\n"
    "s_cmp_eq_u32 s41, 1\n"
    "s_cbranch_scc1 .Lpoller\n"
    "s_mov_b32 s55, 0\n"
    "s_cmp_lg_u32 s41, 0\n"
    "s_cbranch_scc1 .Lend\n"
    "s_lshr_b32 s50, s37, 2\n"
    "s_and_b32 s51, s37, 3\n"
    "s_mov_b32 s52, s43\n"
    "s_lshl1_add_u32 s42, s52, s50\n"
    "s_lshl_b32 s46, s42, 14\n"
    "s_add_u32 s46, s46, 1310720\n"
    "s_lshl_b32 s53, s50, 5\n"
    "s_add_u32 s53, s53, 512\n"
    "s_lshl1_add_u32 s40, s56, 163840\n"
    "s_add_u32 s56, s56, 16\n"
    "s_branch .Lreduce\n"
    ".Lpad_2c_0:\n"
    "s_cbranch_execz .Lpad_2c_1\n"
    ".Lpoller:\n"
    "s_setprio 3\n"
    "s_mov_b64 exec, 0xff\n"
    "v_mov_b32 v48, 0x20000\n"
    "s_mov_b32 s45, 524288\n"
    "v_mov_b64 v[32:33], 0\n"
    "buffer_atomic_add_x2 v[32:33], v3, s[28:31], s40 offen sc0 sc1\n"
    "s_sleep 2\n"
    "v_mov_b64 v[34:35], 0\n"
    "buffer_atomic_add_x2 v[34:35], v3, s[28:31], s40 offen sc0 sc1\n"
    "s_sleep 2\n"
    "v_mov_b64 v[36:37], 0\n"
    "buffer_atomic_add_x2 v[36:37], v3, s[28:31], s40 offen sc0 sc1\n"
    "s_sleep 2\n"
    "v_mov_b64 v[38:39], 0\n"
    "buffer_atomic_add_x2 v[38:39], v3, s[28:31], s40 offen sc0 sc1\n"
    ".Lpoller_l:\n"
    "s_waitcnt vmcnt(3)\n"
    "v_cmp_eq_u64 vcc, s[98:99], v[32:33]\n"
    "s_nop 1\n"
    "v_mov_b32 v49, vcc_lo\n"
    "ds_write_b32 v48, v49\n"
    "s_cmpk_eq_u32 vcc_lo, 0xff\n"
    "s_cbranch_scc1 .Lend\n"
    "v_mov_b64 v[32:33], 0\n"
    "buffer_atomic_add_x2 v[32:33], v3, s[28:31], s40 offen sc0 sc1\n"
    "s_waitcnt vmcnt(3)\n"
    "v_cmp_eq_u64 vcc, s[98:99], v[34:35]\n"
    "s_nop 1\n"
    "v_mov_b32 v49, vcc_lo\n"
    "ds_write_b32 v48, v49\n"
    "s_cmpk_eq_u32 vcc_lo, 0xff\n"
    "s_cbranch_scc1 .Lend\n"
    "v_mov_b64 v[34:35], 0\n"
    "buffer_atomic_add_x2 v[34:35], v3, s[28:31], s40 offen sc0 sc1\n"
    "s_waitcnt vmcnt(3)\n"
    "v_cmp_eq_u64 vcc, s[98:99], v[36:37]\n"
    "s_nop 1\n"
    "v_mov_b32 v49, vcc_lo\n"
    "ds_write_b32 v48, v49\n"
    "s_cmpk_eq_u32 vcc_lo, 0xff\n"
    "s_cbranch_scc1 .Lend\n"
    "v_mov_b64 v[36:37], 0\n"
    "buffer_atomic_add_x2 v[36:37], v3, s[28:31], s40 offen sc0 sc1\n"
    "s_waitcnt vmcnt(3)\n"
    "v_cmp_eq_u64 vcc, s[98:99], v[38:39]\n"
    "s_nop 1\n"
    "v_mov_b32 v49, vcc_lo\n"
    "ds_write_b32 v48, v49\n"
    "s_cmpk_eq_u32 vcc_lo, 0xff\n"
    "s_cbranch_scc1 .Lend\n"
    "v_mov_b64 v[38:39], 0\n"
    "buffer_atomic_add_x2 v[38:39], v3, s[28:31], s40 offen sc0 sc1\n"
    "s_sub_u32 s45, s45, 1\n"
    "s_cbranch_scc0 .Lpoller_l\n"
    "v_mov_b32 v49, 0xff\n"
    "ds_write_b32 v48, v49\n"
    "s_branch .Lend\n"
    ".Lmfma:\n"
    "s_setprio 2\n"
    "v_bfe_u32 v24, v0, 2, 2\n"
    "v_mul_u32_u24 v24, 0x140, v24\n"
    "v_lshl_or_b32 v24, v22, 11, v24\n"
    "v_lshlrev_b32 v25, 1, v0\n"
    "v_and_or_b32 v24, v25, 32, v24\n"
    "v_lshlrev_b32 v3, 3, v0\n"
    "v_and_or_b32 v10, v3, 24, v24\n"
    "v_lshlrev_b32 v2, 4, v0\n"
    "s_bitcmp1_b32 s2, 3\n"
    "s_cbranch_scc1 .Lr2m\n"
    "v_lshrrev_b32 v24, 1, v23\n"
    "v_xor_b32 v24, v24, v22\n"
    "v_lshlrev_b32 v24, 4, v24\n"
    "v_lshl_or_b32 v18, v23, 7, v24\n"
    "s_lshl_b32 s42, s41, 12\n"
    "s_add_u32 s42, s42, 16384\n"
    "v_add_u32 v18, s42, v18\n"
    "v_xor_b32 v19, 64, v18\n"
    "v_mov_b64 v[32:33], 0\n"
    "v_mov_b64 v[34:35], 0\n"
    "v_lshrrev_b32 v24, 5, v0\n"
    "v_xor_b32 v24, v24, v23\n"
    "v_lshlrev_b32 v24, 4, v24\n"
    "v_lshl_or_b32 v9, v23, 8, v24\n"
    "v_lshrrev_b32 v24, 1, v0\n"
    "v_and_or_b32 v9, v24, 8, v9\n"
    "s_lshl_b32 s42, s41, 13\n"
    "s_add_u32 s42, s42, 0x20000\n"
    "v_add_u32 v9, s42, v9\n"
    "s_mov_b32 s42, 0x10000\n"
    "s_mov_b32 s52, 2\n"
    "v_mfma_f32_32x32x16_bf16 v[128:143], v[32:35], v[32:35], 0\n"
    "v_mfma_f32_32x32x16_bf16 v[144:159], v[32:35], v[32:35], 0\n"
    "v_mfma_f32_32x32x16_bf16 v[160:175], v[32:35], v[32:35], 0\n"
    "v_mfma_f32_32x32x16_bf16 v[176:191], v[32:35], v[32:35], 0\n"
    "v_xor_b32 v11, 32, v10\n"
    "v_xor_b32 v12, 64, v10\n"
    "v_xor_b32 v13, 96, v10\n"
    "v_xor_b32 v14, 128, v10\n"
    "v_xor_b32 v15, 160, v10\n"
    "v_xor_b32 v16, 192, v10\n"
    "v_xor_b32 v17, 224, v10\n"
    "v_add_u32 v24, s42, v10\n"
    "v_add_u32 v25, s42, v11\n"
    "v_add_u32 v26, s42, v12\n"
    "v_add_u32 v27, s42, v13\n"
    "v_add_u32 v28, s42, v14\n"
    "v_add_u32 v29, s42, v15\n"
    "v_add_u32 v30, s42, v16\n"
    "v_add_u32 v31, s42, v17\n"
    "v_xor_b32 v224, 32, v9\n"
    "v_xor_b32 v225, 64, v9\n"
    "v_xor_b32 v226, 96, v9\n"
    "v_xor_b32 v227, 128, v9\n"
    "v_xor_b32 v228, 160, v9\n"
    "v_xor_b32 v229, 192, v9\n"
    "v_xor_b32 v230, 224, v9\n"
    "v_add_u32 v20, s42, v18\n"
    "v_add_u32 v21, s42, v19\n"
    "s_lshl2_add_u32 s42, s37, s39\n"
    "s_lshl2_add_u32 s42, s42, s41\n"
    "s_lshl2_add_u32 s42, s42, s38\n"
    "s_lshl_b32 s48, s42, 11\n"
    "s_add_u32 s48, s48, 262144\n"
    "s_lshl_b32 s56, s2, 6\n"
    "s_mov_b32 s50, 0x3e0293ee\n"
    "s_mov_b32 s51, s50\n"
    "s_waitcnt lgkmcnt(0)\n"
    "s_mov_b64 exec, 0\n"
    "s_cmp_eq_u32 s41, 0\n"
    "s_cbranch_scc1 .Lpad_1a_0\n"
    "s_cmp_eq_u32 s41, 1\n"
    "s_cbranch_scc1 .Lpad_1b_0\n"
    "s_cmp_eq_u32 s41, 2\n"
    "s_cbranch_scc1 .Lpad_1c_0\n"
    "s_branch .Lpad_1d_0\n"
    ".Lpadret_1a:\n"
    ".Lpadret_1b:\n"
    ".Lpadret_1c:\n"
    ".Lpadret_1d:\n"
    "s_mov_b64 exec, -1\n"
    "s_barrier\n"
    "ds_read_b128 v[64:67], v18 offset:0\n"
    "ds_read_b64_tr_b16 v[32:33], v10 offset:0\n"
    "ds_read_b128 v[68:71], v18 offset:2048\n"
    "ds_read_b64_tr_b16 v[34:35], v10 offset:1024\n"
    "ds_read_b64_tr_b16 v[36:37], v11 offset:0\n"
    "ds_read_b64_tr_b16 v[38:39], v11 offset:1024\n"
    "ds_read_b64_tr_b16 v[40:41], v12 offset:0\n"
    "ds_read_b64_tr_b16 v[42:43], v12 offset:1024\n"
    "ds_read_b64_tr_b16 v[44:45], v13 offset:0\n"
    "ds_read_b64_tr_b16 v[46:47], v13 offset:1024\n"
    "ds_read_b64_tr_b16 v[48:49], v14 offset:0\n"
    "ds_read_b64_tr_b16 v[50:51], v14 offset:1024\n"
    "ds_read_b64_tr_b16 v[52:53], v15 offset:0\n"
    "ds_read_b64_tr_b16 v[54:55], v15 offset:1024\n"
    ".Lr1m_loop:\n"
    "s_waitcnt lgkmcnt(8)\n"
    "v_mfma_f32_16x16x32_bf16 v[128:131], v[32:35], v[64:67], v[128:131]\n"
    "ds_read_b64_tr_b16 v[56:57], v16 offset:0\n"
    "v_mfma_f32_16x16x32_bf16 v[132:135], v[32:35], v[68:71], v[132:135]\n"
    "ds_read_b64_tr_b16 v[58:59], v16 offset:1024\n"
    "v_mfma_f32_16x16x32_bf16 v[136:139], v[36:39], v[64:67], v[136:139]\n"
    "ds_read_b64_tr_b16 v[60:61], v17 offset:0\n"
    "v_mfma_f32_16x16x32_bf16 v[140:143], v[36:39], v[68:71], v[140:143]\n"
    "ds_read_b64_tr_b16 v[62:63], v17 offset:1024\n"
    "s_cmp_eq_u32 s52, 0\n"
    "s_waitcnt lgkmcnt(8)\n"
    "v_mfma_f32_16x16x32_bf16 v[144:147], v[40:43], v[64:67], v[144:147]\n"
    "ds_read_b128 v[72:75], v19 offset:0\n"
    "ds_read_b64_tr_b16 v[32:33], v10 offset:8192\n"
    "v_mfma_f32_16x16x32_bf16 v[148:151], v[40:43], v[68:71], v[148:151]\n"
    "ds_read_b128 v[76:79], v19 offset:2048\n"
    "ds_read_b64_tr_b16 v[34:35], v10 offset:9216\n"
    "v_mfma_f32_16x16x32_bf16 v[152:155], v[44:47], v[64:67], v[152:155]\n"
    "ds_read_b64_tr_b16 v[36:37], v11 offset:8192\n"
    "v_mfma_f32_16x16x32_bf16 v[156:159], v[44:47], v[68:71], v[156:159]\n"
    "ds_read_b64_tr_b16 v[38:39], v11 offset:9216\n"
    "s_waitcnt lgkmcnt(10)\n"
    "v_mfma_f32_16x16x32_bf16 v[160:163], v[48:51], v[64:67], v[160:163]\n"
    "ds_read_b64_tr_b16 v[40:41], v12 offset:8192\n"
    "v_mfma_f32_16x16x32_bf16 v[164:167], v[48:51], v[68:71], v[164:167]\n"
    "ds_read_b64_tr_b16 v[42:43], v12 offset:9216\n"
    "v_mfma_f32_16x16x32_bf16 v[168:171], v[52:55], v[64:67], v[168:171]\n"
    "ds_read_b64_tr_b16 v[44:45], v13 offset:8192\n"
    "v_mfma_f32_16x16x32_bf16 v[172:175], v[52:55], v[68:71], v[172:175]\n"
    "ds_read_b64_tr_b16 v[46:47], v13 offset:9216\n"
    "s_waitcnt lgkmcnt(10)\n"
    "v_mfma_f32_16x16x32_bf16 v[176:179], v[56:59], v[64:67], v[176:179]\n"
    "ds_read_b64_tr_b16 v[48:49], v14 offset:8192\n"
    "v_mfma_f32_16x16x32_bf16 v[180:183], v[56:59], v[68:71], v[180:183]\n"
    "ds_read_b64_tr_b16 v[50:51], v14 offset:9216\n"
    "v_mfma_f32_16x16x32_bf16 v[184:187], v[60:63], v[64:67], v[184:187]\n"
    "ds_read_b64_tr_b16 v[52:53], v15 offset:8192\n"
    "v_mfma_f32_16x16x32_bf16 v[188:191], v[60:63], v[68:71], v[188:191]\n"
    "ds_read_b64_tr_b16 v[54:55], v15 offset:9216\n"
    "s_waitcnt lgkmcnt(8)\n"
    "v_mfma_f32_16x16x32_bf16 v[128:131], v[32:35], v[72:75], v[128:131]\n"
    "ds_read_b64_tr_b16 v[56:57], v16 offset:8192\n"
    "v_mfma_f32_16x16x32_bf16 v[132:135], v[32:35], v[76:79], v[132:135]\n"
    "ds_read_b64_tr_b16 v[58:59], v16 offset:9216\n"
    "v_mfma_f32_16x16x32_bf16 v[136:139], v[36:39], v[72:75], v[136:139]\n"
    "ds_read_b64_tr_b16 v[60:61], v17 offset:8192\n"
    "v_mfma_f32_16x16x32_bf16 v[140:143], v[36:39], v[76:79], v[140:143]\n"
    "ds_read_b64_tr_b16 v[62:63], v17 offset:9216\n"
    "s_cbranch_scc1 .Lr1m_tail\n"
    "s_barrier\n"
    "s_waitcnt lgkmcnt(8)\n"
    "v_mfma_f32_16x16x32_bf16 v[144:147], v[40:43], v[72:75], v[144:147]\n"
    "ds_read_b128 v[64:67], v18 offset:32768\n"
    "ds_read_b64_tr_b16 v[32:33], v10 offset:32768\n"
    "v_mfma_f32_16x16x32_bf16 v[148:151], v[40:43], v[76:79], v[148:151]\n"
    "ds_read_b128 v[68:71], v18 offset:34816\n"
    "ds_read_b64_tr_b16 v[34:35], v10 offset:33792\n"
    "v_mfma_f32_16x16x32_bf16 v[152:155], v[44:47], v[72:75], v[152:155]\n"
    "ds_read_b64_tr_b16 v[36:37], v11 offset:32768\n"
    "v_mfma_f32_16x16x32_bf16 v[156:159], v[44:47], v[76:79], v[156:159]\n"
    "ds_read_b64_tr_b16 v[38:39], v11 offset:33792\n"
    "s_waitcnt lgkmcnt(10)\n"
    "v_mfma_f32_16x16x32_bf16 v[160:163], v[48:51], v[72:75], v[160:163]\n"
    "ds_read_b64_tr_b16 v[40:41], v12 offset:32768\n"
    "v_mfma_f32_16x16x32_bf16 v[164:167], v[48:51], v[76:79], v[164:167]\n"
    "ds_read_b64_tr_b16 v[42:43], v12 offset:33792\n"
    "v_mfma_f32_16x16x32_bf16 v[168:171], v[52:55], v[72:75], v[168:171]\n"
    "ds_read_b64_tr_b16 v[44:45], v13 offset:32768\n"
    "v_mfma_f32_16x16x32_bf16 v[172:175], v[52:55], v[76:79], v[172:175]\n"
    "ds_read_b64_tr_b16 v[46:47], v13 offset:33792\n"
    "s_waitcnt lgkmcnt(10)\n"
    "v_mfma_f32_16x16x32_bf16 v[176:179], v[56:59], v[72:75], v[176:179]\n"
    "ds_read_b64_tr_b16 v[48:49], v14 offset:32768\n"
    "v_mfma_f32_16x16x32_bf16 v[180:183], v[56:59], v[76:79], v[180:183]\n"
    "ds_read_b64_tr_b16 v[50:51], v14 offset:33792\n"
    "v_mfma_f32_16x16x32_bf16 v[184:187], v[60:63], v[72:75], v[184:187]\n"
    "ds_read_b64_tr_b16 v[52:53], v15 offset:32768\n"
    "v_mfma_f32_16x16x32_bf16 v[188:191], v[60:63], v[76:79], v[188:191]\n"
    "ds_read_b64_tr_b16 v[54:55], v15 offset:33792\n"
    "s_waitcnt lgkmcnt(8)\n"
    "v_mfma_f32_16x16x32_bf16 v[128:131], v[32:35], v[64:67], v[128:131]\n"
    "ds_read_b64_tr_b16 v[56:57], v16 offset:32768\n"
    "v_mfma_f32_16x16x32_bf16 v[132:135], v[32:35], v[68:71], v[132:135]\n"
    "ds_read_b64_tr_b16 v[58:59], v16 offset:33792\n"
    "v_mfma_f32_16x16x32_bf16 v[136:139], v[36:39], v[64:67], v[136:139]\n"
    "ds_read_b64_tr_b16 v[60:61], v17 offset:32768\n"
    "v_mfma_f32_16x16x32_bf16 v[140:143], v[36:39], v[68:71], v[140:143]\n"
    "ds_read_b64_tr_b16 v[62:63], v17 offset:33792\n"
    "s_waitcnt lgkmcnt(8)\n"
    "v_mfma_f32_16x16x32_bf16 v[144:147], v[40:43], v[64:67], v[144:147]\n"
    "ds_read_b128 v[72:75], v19 offset:32768\n"
    "ds_read_b64_tr_b16 v[32:33], v10 offset:40960\n"
    "v_mfma_f32_16x16x32_bf16 v[148:151], v[40:43], v[68:71], v[148:151]\n"
    "ds_read_b128 v[76:79], v19 offset:34816\n"
    "ds_read_b64_tr_b16 v[34:35], v10 offset:41984\n"
    "v_mfma_f32_16x16x32_bf16 v[152:155], v[44:47], v[64:67], v[152:155]\n"
    "ds_read_b64_tr_b16 v[36:37], v11 offset:40960\n"
    "v_mfma_f32_16x16x32_bf16 v[156:159], v[44:47], v[68:71], v[156:159]\n"
    "ds_read_b64_tr_b16 v[38:39], v11 offset:41984\n"
    "s_waitcnt lgkmcnt(10)\n"
    "v_mfma_f32_16x16x32_bf16 v[160:163], v[48:51], v[64:67], v[160:163]\n"
    "ds_read_b64_tr_b16 v[40:41], v12 offset:40960\n"
    "v_mfma_f32_16x16x32_bf16 v[164:167], v[48:51], v[68:71], v[164:167]\n"
    "ds_read_b64_tr_b16 v[42:43], v12 offset:41984\n"
    "v_mfma_f32_16x16x32_bf16 v[168:171], v[52:55], v[64:67], v[168:171]\n"
    "ds_read_b64_tr_b16 v[44:45], v13 offset:40960\n"
    "v_mfma_f32_16x16x32_bf16 v[172:175], v[52:55], v[68:71], v[172:175]\n"
    "ds_read_b64_tr_b16 v[46:47], v13 offset:41984\n"
    "s_waitcnt lgkmcnt(10)\n"
    "v_mfma_f32_16x16x32_bf16 v[176:179], v[56:59], v[64:67], v[176:179]\n"
    "ds_read_b64_tr_b16 v[48:49], v14 offset:40960\n"
    "v_mfma_f32_16x16x32_bf16 v[180:183], v[56:59], v[68:71], v[180:183]\n"
    "ds_read_b64_tr_b16 v[50:51], v14 offset:41984\n"
    "v_mfma_f32_16x16x32_bf16 v[184:187], v[60:63], v[64:67], v[184:187]\n"
    "ds_read_b64_tr_b16 v[52:53], v15 offset:40960\n"
    "v_mfma_f32_16x16x32_bf16 v[188:191], v[60:63], v[68:71], v[188:191]\n"
    "ds_read_b64_tr_b16 v[54:55], v15 offset:41984\n"
    "s_waitcnt lgkmcnt(8)\n"
    "v_mfma_f32_16x16x32_bf16 v[128:131], v[32:35], v[72:75], v[128:131]\n"
    "ds_read_b64_tr_b16 v[56:57], v16 offset:40960\n"
    "v_mfma_f32_16x16x32_bf16 v[132:135], v[32:35], v[76:79], v[132:135]\n"
    "ds_read_b64_tr_b16 v[58:59], v16 offset:41984\n"
    "v_mfma_f32_16x16x32_bf16 v[136:139], v[36:39], v[72:75], v[136:139]\n"
    "ds_read_b64_tr_b16 v[60:61], v17 offset:40960\n"
    "v_mfma_f32_16x16x32_bf16 v[140:143], v[36:39], v[76:79], v[140:143]\n"
    "ds_read_b64_tr_b16 v[62:63], v17 offset:41984\n"
    "s_barrier\n"
    "s_waitcnt lgkmcnt(8)\n"
    "v_mfma_f32_16x16x32_bf16 v[144:147], v[40:43], v[72:75], v[144:147]\n"
    "ds_read_b128 v[64:67], v20 offset:0\n"
    "ds_read_b64_tr_b16 v[32:33], v24 offset:0\n"
    "v_mfma_f32_16x16x32_bf16 v[148:151], v[40:43], v[76:79], v[148:151]\n"
    "ds_read_b128 v[68:71], v20 offset:2048\n"
    "ds_read_b64_tr_b16 v[34:35], v24 offset:1024\n"
    "v_mfma_f32_16x16x32_bf16 v[152:155], v[44:47], v[72:75], v[152:155]\n"
    "ds_read_b64_tr_b16 v[36:37], v25 offset:0\n"
    "v_mfma_f32_16x16x32_bf16 v[156:159], v[44:47], v[76:79], v[156:159]\n"
    "ds_read_b64_tr_b16 v[38:39], v25 offset:1024\n"
    "s_waitcnt lgkmcnt(10)\n"
    "v_mfma_f32_16x16x32_bf16 v[160:163], v[48:51], v[72:75], v[160:163]\n"
    "ds_read_b64_tr_b16 v[40:41], v26 offset:0\n"
    "v_mfma_f32_16x16x32_bf16 v[164:167], v[48:51], v[76:79], v[164:167]\n"
    "ds_read_b64_tr_b16 v[42:43], v26 offset:1024\n"
    "v_mfma_f32_16x16x32_bf16 v[168:171], v[52:55], v[72:75], v[168:171]\n"
    "ds_read_b64_tr_b16 v[44:45], v27 offset:0\n"
    "v_mfma_f32_16x16x32_bf16 v[172:175], v[52:55], v[76:79], v[172:175]\n"
    "ds_read_b64_tr_b16 v[46:47], v27 offset:1024\n"
    "s_waitcnt lgkmcnt(10)\n"
    "v_mfma_f32_16x16x32_bf16 v[176:179], v[56:59], v[72:75], v[176:179]\n"
    "ds_read_b64_tr_b16 v[48:49], v28 offset:0\n"
    "v_mfma_f32_16x16x32_bf16 v[180:183], v[56:59], v[76:79], v[180:183]\n"
    "ds_read_b64_tr_b16 v[50:51], v28 offset:1024\n"
    "v_mfma_f32_16x16x32_bf16 v[184:187], v[60:63], v[72:75], v[184:187]\n"
    "ds_read_b64_tr_b16 v[52:53], v29 offset:0\n"
    "v_mfma_f32_16x16x32_bf16 v[188:191], v[60:63], v[76:79], v[188:191]\n"
    "ds_read_b64_tr_b16 v[54:55], v29 offset:1024\n"
    "s_waitcnt lgkmcnt(8)\n"
    "v_mfma_f32_16x16x32_bf16 v[128:131], v[32:35], v[64:67], v[128:131]\n"
    "ds_read_b64_tr_b16 v[56:57], v30 offset:0\n"
    "v_mfma_f32_16x16x32_bf16 v[132:135], v[32:35], v[68:71], v[132:135]\n"
    "ds_read_b64_tr_b16 v[58:59], v30 offset:1024\n"
    "v_mfma_f32_16x16x32_bf16 v[136:139], v[36:39], v[64:67], v[136:139]\n"
    "ds_read_b64_tr_b16 v[60:61], v31 offset:0\n"
    "v_mfma_f32_16x16x32_bf16 v[140:143], v[36:39], v[68:71], v[140:143]\n"
    "ds_read_b64_tr_b16 v[62:63], v31 offset:1024\n"
    "s_waitcnt lgkmcnt(8)\n"
    "v_mfma_f32_16x16x32_bf16 v[144:147], v[40:43], v[64:67], v[144:147]\n"
    "ds_read_b128 v[72:75], v21 offset:0\n"
    "ds_read_b64_tr_b16 v[32:33], v24 offset:8192\n"
    "v_mfma_f32_16x16x32_bf16 v[148:151], v[40:43], v[68:71], v[148:151]\n"
    "ds_read_b128 v[76:79], v21 offset:2048\n"
    "ds_read_b64_tr_b16 v[34:35], v24 offset:9216\n"
    "v_mfma_f32_16x16x32_bf16 v[152:155], v[44:47], v[64:67], v[152:155]\n"
    "ds_read_b64_tr_b16 v[36:37], v25 offset:8192\n"
    "v_mfma_f32_16x16x32_bf16 v[156:159], v[44:47], v[68:71], v[156:159]\n"
    "ds_read_b64_tr_b16 v[38:39], v25 offset:9216\n"
    "s_waitcnt lgkmcnt(10)\n"
    "v_mfma_f32_16x16x32_bf16 v[160:163], v[48:51], v[64:67], v[160:163]\n"
    "ds_read_b64_tr_b16 v[40:41], v26 offset:8192\n"
    "v_mfma_f32_16x16x32_bf16 v[164:167], v[48:51], v[68:71], v[164:167]\n"
    "ds_read_b64_tr_b16 v[42:43], v26 offset:9216\n"
    "v_mfma_f32_16x16x32_bf16 v[168:171], v[52:55], v[64:67], v[168:171]\n"
    "ds_read_b64_tr_b16 v[44:45], v27 offset:8192\n"
    "v_mfma_f32_16x16x32_bf16 v[172:175], v[52:55], v[68:71], v[172:175]\n"
    "ds_read_b64_tr_b16 v[46:47], v27 offset:9216\n"
    "s_waitcnt lgkmcnt(10)\n"
    "v_mfma_f32_16x16x32_bf16 v[176:179], v[56:59], v[64:67], v[176:179]\n"
    "ds_read_b64_tr_b16 v[48:49], v28 offset:8192\n"
    "v_mfma_f32_16x16x32_bf16 v[180:183], v[56:59], v[68:71], v[180:183]\n"
    "ds_read_b64_tr_b16 v[50:51], v28 offset:9216\n"
    "v_mfma_f32_16x16x32_bf16 v[184:187], v[60:63], v[64:67], v[184:187]\n"
    "ds_read_b64_tr_b16 v[52:53], v29 offset:8192\n"
    "v_mfma_f32_16x16x32_bf16 v[188:191], v[60:63], v[68:71], v[188:191]\n"
    "ds_read_b64_tr_b16 v[54:55], v29 offset:9216\n"
    "s_waitcnt lgkmcnt(8)\n"
    "v_mfma_f32_16x16x32_bf16 v[128:131], v[32:35], v[72:75], v[128:131]\n"
    "ds_read_b64_tr_b16 v[56:57], v30 offset:8192\n"
    "v_mfma_f32_16x16x32_bf16 v[132:135], v[32:35], v[76:79], v[132:135]\n"
    "ds_read_b64_tr_b16 v[58:59], v30 offset:9216\n"
    "v_mfma_f32_16x16x32_bf16 v[136:139], v[36:39], v[72:75], v[136:139]\n"
    "ds_read_b64_tr_b16 v[60:61], v31 offset:8192\n"
    "v_mfma_f32_16x16x32_bf16 v[140:143], v[36:39], v[76:79], v[140:143]\n"
    "ds_read_b64_tr_b16 v[62:63], v31 offset:9216\n"
    "s_barrier\n"
    "s_waitcnt lgkmcnt(8)\n"
    "v_mfma_f32_16x16x32_bf16 v[144:147], v[40:43], v[72:75], v[144:147]\n"
    "ds_read_b128 v[64:67], v20 offset:32768\n"
    "ds_read_b64_tr_b16 v[32:33], v24 offset:32768\n"
    "v_mfma_f32_16x16x32_bf16 v[148:151], v[40:43], v[76:79], v[148:151]\n"
    "ds_read_b128 v[68:71], v20 offset:34816\n"
    "ds_read_b64_tr_b16 v[34:35], v24 offset:33792\n"
    "v_mfma_f32_16x16x32_bf16 v[152:155], v[44:47], v[72:75], v[152:155]\n"
    "ds_read_b64_tr_b16 v[36:37], v25 offset:32768\n"
    "v_mfma_f32_16x16x32_bf16 v[156:159], v[44:47], v[76:79], v[156:159]\n"
    "ds_read_b64_tr_b16 v[38:39], v25 offset:33792\n"
    "s_waitcnt lgkmcnt(10)\n"
    "v_mfma_f32_16x16x32_bf16 v[160:163], v[48:51], v[72:75], v[160:163]\n"
    "ds_read_b64_tr_b16 v[40:41], v26 offset:32768\n"
    "v_mfma_f32_16x16x32_bf16 v[164:167], v[48:51], v[76:79], v[164:167]\n"
    "ds_read_b64_tr_b16 v[42:43], v26 offset:33792\n"
    "v_mfma_f32_16x16x32_bf16 v[168:171], v[52:55], v[72:75], v[168:171]\n"
    "ds_read_b64_tr_b16 v[44:45], v27 offset:32768\n"
    "v_mfma_f32_16x16x32_bf16 v[172:175], v[52:55], v[76:79], v[172:175]\n"
    "ds_read_b64_tr_b16 v[46:47], v27 offset:33792\n"
    "s_waitcnt lgkmcnt(10)\n"
    "v_mfma_f32_16x16x32_bf16 v[176:179], v[56:59], v[72:75], v[176:179]\n"
    "ds_read_b64_tr_b16 v[48:49], v28 offset:32768\n"
    "v_mfma_f32_16x16x32_bf16 v[180:183], v[56:59], v[76:79], v[180:183]\n"
    "ds_read_b64_tr_b16 v[50:51], v28 offset:33792\n"
    "v_mfma_f32_16x16x32_bf16 v[184:187], v[60:63], v[72:75], v[184:187]\n"
    "ds_read_b64_tr_b16 v[52:53], v29 offset:32768\n"
    "v_mfma_f32_16x16x32_bf16 v[188:191], v[60:63], v[76:79], v[188:191]\n"
    "ds_read_b64_tr_b16 v[54:55], v29 offset:33792\n"
    "s_waitcnt lgkmcnt(8)\n"
    "v_mfma_f32_16x16x32_bf16 v[128:131], v[32:35], v[64:67], v[128:131]\n"
    "ds_read_b64_tr_b16 v[56:57], v30 offset:32768\n"
    "v_mfma_f32_16x16x32_bf16 v[132:135], v[32:35], v[68:71], v[132:135]\n"
    "ds_read_b64_tr_b16 v[58:59], v30 offset:33792\n"
    "v_mfma_f32_16x16x32_bf16 v[136:139], v[36:39], v[64:67], v[136:139]\n"
    "ds_read_b64_tr_b16 v[60:61], v31 offset:32768\n"
    "v_mfma_f32_16x16x32_bf16 v[140:143], v[36:39], v[68:71], v[140:143]\n"
    "ds_read_b64_tr_b16 v[62:63], v31 offset:33792\n"
    "s_waitcnt lgkmcnt(8)\n"
    "v_mfma_f32_16x16x32_bf16 v[144:147], v[40:43], v[64:67], v[144:147]\n"
    "ds_read_b128 v[72:75], v21 offset:32768\n"
    "ds_read_b64_tr_b16 v[32:33], v24 offset:40960\n"
    "v_mfma_f32_16x16x32_bf16 v[148:151], v[40:43], v[68:71], v[148:151]\n"
    "ds_read_b128 v[76:79], v21 offset:34816\n"
    "ds_read_b64_tr_b16 v[34:35], v24 offset:41984\n"
    "s_sub_u32 s52, s52, 1\n"
    "v_mfma_f32_16x16x32_bf16 v[152:155], v[44:47], v[64:67], v[152:155]\n"
    "ds_read_b64_tr_b16 v[36:37], v25 offset:40960\n"
    "v_mfma_f32_16x16x32_bf16 v[156:159], v[44:47], v[68:71], v[156:159]\n"
    "ds_read_b64_tr_b16 v[38:39], v25 offset:41984\n"
    "s_waitcnt lgkmcnt(10)\n"
    "v_mfma_f32_16x16x32_bf16 v[160:163], v[48:51], v[64:67], v[160:163]\n"
    "ds_read_b64_tr_b16 v[40:41], v26 offset:40960\n"
    "v_mfma_f32_16x16x32_bf16 v[164:167], v[48:51], v[68:71], v[164:167]\n"
    "ds_read_b64_tr_b16 v[42:43], v26 offset:41984\n"
    "v_mfma_f32_16x16x32_bf16 v[168:171], v[52:55], v[64:67], v[168:171]\n"
    "ds_read_b64_tr_b16 v[44:45], v27 offset:40960\n"
    "v_mfma_f32_16x16x32_bf16 v[172:175], v[52:55], v[68:71], v[172:175]\n"
    "ds_read_b64_tr_b16 v[46:47], v27 offset:41984\n"
    "s_waitcnt lgkmcnt(10)\n"
    "v_mfma_f32_16x16x32_bf16 v[176:179], v[56:59], v[64:67], v[176:179]\n"
    "ds_read_b64_tr_b16 v[48:49], v28 offset:40960\n"
    "v_mfma_f32_16x16x32_bf16 v[180:183], v[56:59], v[68:71], v[180:183]\n"
    "ds_read_b64_tr_b16 v[50:51], v28 offset:41984\n"
    "v_mfma_f32_16x16x32_bf16 v[184:187], v[60:63], v[64:67], v[184:187]\n"
    "ds_read_b64_tr_b16 v[52:53], v29 offset:40960\n"
    "v_mfma_f32_16x16x32_bf16 v[188:191], v[60:63], v[68:71], v[188:191]\n"
    "ds_read_b64_tr_b16 v[54:55], v29 offset:41984\n"
    "s_waitcnt lgkmcnt(8)\n"
    "v_mfma_f32_16x16x32_bf16 v[128:131], v[32:35], v[72:75], v[128:131]\n"
    "ds_read_b64_tr_b16 v[56:57], v30 offset:40960\n"
    "v_mfma_f32_16x16x32_bf16 v[132:135], v[32:35], v[76:79], v[132:135]\n"
    "ds_read_b64_tr_b16 v[58:59], v30 offset:41984\n"
    "v_mfma_f32_16x16x32_bf16 v[136:139], v[36:39], v[72:75], v[136:139]\n"
    "ds_read_b64_tr_b16 v[60:61], v31 offset:40960\n"
    "v_mfma_f32_16x16x32_bf16 v[140:143], v[36:39], v[76:79], v[140:143]\n"
    "ds_read_b64_tr_b16 v[62:63], v31 offset:41984\n"
    "s_barrier\n"
    "s_waitcnt lgkmcnt(8)\n"
    "v_mfma_f32_16x16x32_bf16 v[144:147], v[40:43], v[72:75], v[144:147]\n"
    "ds_read_b128 v[64:67], v18 offset:0\n"
    "ds_read_b64_tr_b16 v[32:33], v10 offset:0\n"
    "v_mfma_f32_16x16x32_bf16 v[148:151], v[40:43], v[76:79], v[148:151]\n"
    "ds_read_b128 v[68:71], v18 offset:2048\n"
    "ds_read_b64_tr_b16 v[34:35], v10 offset:1024\n"
    "v_mfma_f32_16x16x32_bf16 v[152:155], v[44:47], v[72:75], v[152:155]\n"
    "ds_read_b64_tr_b16 v[36:37], v11 offset:0\n"
    "v_mfma_f32_16x16x32_bf16 v[156:159], v[44:47], v[76:79], v[156:159]\n"
    "ds_read_b64_tr_b16 v[38:39], v11 offset:1024\n"
    "s_waitcnt lgkmcnt(10)\n"
    "v_mfma_f32_16x16x32_bf16 v[160:163], v[48:51], v[72:75], v[160:163]\n"
    "ds_read_b64_tr_b16 v[40:41], v12 offset:0\n"
    "v_mfma_f32_16x16x32_bf16 v[164:167], v[48:51], v[76:79], v[164:167]\n"
    "ds_read_b64_tr_b16 v[42:43], v12 offset:1024\n"
    "v_mfma_f32_16x16x32_bf16 v[168:171], v[52:55], v[72:75], v[168:171]\n"
    "ds_read_b64_tr_b16 v[44:45], v13 offset:0\n"
    "v_mfma_f32_16x16x32_bf16 v[172:175], v[52:55], v[76:79], v[172:175]\n"
    "ds_read_b64_tr_b16 v[46:47], v13 offset:1024\n"
    "s_waitcnt lgkmcnt(10)\n"
    "v_mfma_f32_16x16x32_bf16 v[176:179], v[56:59], v[72:75], v[176:179]\n"
    "ds_read_b64_tr_b16 v[48:49], v14 offset:0\n"
    "v_mfma_f32_16x16x32_bf16 v[180:183], v[56:59], v[76:79], v[180:183]\n"
    "ds_read_b64_tr_b16 v[50:51], v14 offset:1024\n"
    "v_mfma_f32_16x16x32_bf16 v[184:187], v[60:63], v[72:75], v[184:187]\n"
    "ds_read_b64_tr_b16 v[52:53], v15 offset:0\n"
    "v_mfma_f32_16x16x32_bf16 v[188:191], v[60:63], v[76:79], v[188:191]\n"
    "ds_read_b64_tr_b16 v[54:55], v15 offset:1024\n"
    "s_branch .Lr1m_loop\n"
    ".Lpad_1c_0:\n"
    "s_cbranch_execz .Lpadret_1c\n"
    ".Lr1m_tail:\n"
    "s_waitcnt lgkmcnt(10)\n"
    "v_mfma_f32_16x16x32_bf16 v[144:147], v[40:43], v[72:75], v[144:147]\n"
    "v_mfma_f32_16x16x32_bf16 v[148:151], v[40:43], v[76:79], v[148:151]\n"
    "v_cvt_pk_bf16_f32 v192, v128, v129\n"
    "v_cvt_pk_bf16_f32 v196, v132, v133\n"
    "s_waitcnt lgkmcnt(8)\n"
    "v_mfma_f32_16x16x32_bf16 v[152:155], v[44:47], v[72:75], v[152:155]\n"
    "v_cvt_pk_bf16_f32 v193, v130, v131\n"
    "v_cvt_pk_bf16_f32 v197, v134, v135\n"
    "v_mfma_f32_16x16x32_bf16 v[156:159], v[44:47], v[76:79], v[156:159]\n"
    "v_cvt_pk_bf16_f32 v194, v136, v137\n"
    "v_cvt_pk_bf16_f32 v198, v140, v141\n"
    "s_waitcnt lgkmcnt(6)\n"
    "v_mfma_f32_16x16x32_bf16 v[160:163], v[48:51], v[72:75], v[160:163]\n"
    "v_cvt_pk_bf16_f32 v195, v138, v139\n"
    "v_cvt_pk_bf16_f32 v199, v142, v143\n"
    "v_mfma_f32_16x16x32_bf16 v[164:167], v[48:51], v[76:79], v[164:167]\n"
    "v_cvt_pk_bf16_f32 v200, v144, v145\n"
    "v_cvt_pk_bf16_f32 v204, v148, v149\n"
    "s_waitcnt lgkmcnt(4)\n"
    "v_mfma_f32_16x16x32_bf16 v[168:171], v[52:55], v[72:75], v[168:171]\n"
    "v_cvt_pk_bf16_f32 v201, v146, v147\n"
    "v_cvt_pk_bf16_f32 v205, v150, v151\n"
    "v_mfma_f32_16x16x32_bf16 v[172:175], v[52:55], v[76:79], v[172:175]\n"
    "v_cvt_pk_bf16_f32 v202, v152, v153\n"
    "v_cvt_pk_bf16_f32 v206, v156, v157\n"
    "s_waitcnt lgkmcnt(2)\n"
    "v_mfma_f32_16x16x32_bf16 v[176:179], v[56:59], v[72:75], v[176:179]\n"
    "v_cvt_pk_bf16_f32 v203, v154, v155\n"
    "v_cvt_pk_bf16_f32 v207, v158, v159\n"
    "v_mfma_f32_16x16x32_bf16 v[180:183], v[56:59], v[76:79], v[180:183]\n"
    "v_cvt_pk_bf16_f32 v208, v160, v161\n"
    "v_cvt_pk_bf16_f32 v212, v164, v165\n"
    "s_waitcnt lgkmcnt(0)\n"
    "v_mfma_f32_16x16x32_bf16 v[184:187], v[60:63], v[72:75], v[184:187]\n"
    "v_cvt_pk_bf16_f32 v209, v162, v163\n"
    "v_cvt_pk_bf16_f32 v213, v166, v167\n"
    "v_mfma_f32_16x16x32_bf16 v[188:191], v[60:63], v[76:79], v[188:191]\n"
    "v_cvt_pk_bf16_f32 v210, v168, v169\n"
    "v_cvt_pk_bf16_f32 v214, v172, v173\n"
    "v_cvt_pk_bf16_f32 v211, v170, v171\n"
    "v_cvt_pk_bf16_f32 v215, v174, v175\n"
    "s_barrier\n"
    "ds_read_b64 v[80:81], v9 offset:0\n"
    "ds_read_b64 v[84:85], v9 offset:4096\n"
    "ds_read_b64 v[82:83], v224 offset:0\n"
    "ds_read_b64 v[86:87], v224 offset:4096\n"
    "ds_read_b64 v[88:89], v225 offset:0\n"
    "ds_read_b64 v[92:93], v225 offset:4096\n"
    "ds_read_b64 v[90:91], v226 offset:0\n"
    "ds_read_b64 v[94:95], v226 offset:4096\n"
    "v_cvt_pk_bf16_f32 v216, v176, v177\n"
    "ds_read_b64 v[96:97], v227 offset:0\n"
    "v_cvt_pk_bf16_f32 v220, v180, v181\n"
    "ds_read_b64 v[100:101], v227 offset:4096\n"
    "v_cvt_pk_bf16_f32 v217, v178, v179\n"
    "ds_read_b64 v[98:99], v228 offset:0\n"
    "v_cvt_pk_bf16_f32 v221, v182, v183\n"
    "ds_read_b64 v[102:103], v228 offset:4096\n"
    "v_cvt_pk_bf16_f32 v218, v184, v185\n"
    "ds_read_b64 v[104:105], v229 offset:0\n"
    "v_cvt_pk_bf16_f32 v222, v188, v189\n"
    "ds_read_b64 v[108:109], v229 offset:4096\n"
    "v_cvt_pk_bf16_f32 v219, v186, v187\n"
    "ds_read_b64 v[106:107], v230 offset:0\n"
    "v_cvt_pk_bf16_f32 v223, v190, v191\n"
    "s_waitcnt lgkmcnt(11)\n"
    "v_mfma_f32_16x16x32_bf16 v[112:115], v[80:83], v[192:195], 0\n"
    "ds_read_b64 v[110:111], v230 offset:4096\n"
    "v_mfma_f32_16x16x32_bf16 v[116:119], v[84:87], v[192:195], 0\n"
    "v_mfma_f32_16x16x32_bf16 v[120:123], v[80:83], v[196:199], 0\n"
    "v_mfma_f32_16x16x32_bf16 v[124:127], v[84:87], v[196:199], 0\n"
    "s_waitcnt lgkmcnt(8)\n"
    "v_mfma_f32_16x16x32_bf16 v[112:115], v[88:91], v[200:203], v[112:115]\n"
    "v_mfma_f32_16x16x32_bf16 v[116:119], v[92:95], v[200:203], v[116:119]\n"
    "v_mfma_f32_16x16x32_bf16 v[120:123], v[88:91], v[204:207], v[120:123]\n"
    "v_mfma_f32_16x16x32_bf16 v[124:127], v[92:95], v[204:207], v[124:127]\n"
    "s_waitcnt lgkmcnt(4)\n"
    "v_mfma_f32_16x16x32_bf16 v[112:115], v[96:99], v[208:211], v[112:115]\n"
    "v_mfma_f32_16x16x32_bf16 v[116:119], v[100:103], v[208:211], v[116:119]\n"
    "v_mfma_f32_16x16x32_bf16 v[120:123], v[96:99], v[212:215], v[120:123]\n"
    "v_mfma_f32_16x16x32_bf16 v[124:127], v[100:103], v[212:215], v[124:127]\n"
    "s_waitcnt lgkmcnt(0)\n"
    "v_mfma_f32_16x16x32_bf16 v[112:115], v[104:107], v[216:219], v[112:115]\n"
    "v_mfma_f32_16x16x32_bf16 v[116:119], v[108:111], v[216:219], v[116:119]\n"
    "v_mfma_f32_16x16x32_bf16 v[120:123], v[104:107], v[220:223], v[120:123]\n"
    "v_mfma_f32_16x16x32_bf16 v[124:127], v[108:111], v[220:223], v[124:127]\n"
    "s_nop 5\n"
    "v_max3_f32 v32, v112, v113, v114\n"
    "v_max3_f32 v34, v115, v116, v117\n"
    "v_max3_f32 v32, v32, v118, v119\n"
    "v_max_f32 v32, v32, v34\n"
    "v_max3_f32 v33, v120, v121, v122\n"
    "v_max3_f32 v35, v123, v124, v125\n"
    "v_max3_f32 v33, v33, v126, v127\n"
    "v_max_f32 v33, v33, v35\n"
    "s_nop 1\n"
    "v_permlane32_swap_b32 v32, v33\n"
    "v_max_f32 v32, v32, v33\n"
    "v_mov_b32 v33, v32\n"
    "s_nop 1\n"
    "v_permlane16_swap_b32 v32, v33\n"
    "v_max_f32 v32, v32, v33\n"
    "v_mul_f32 v32, s50, v32\n"
    "v_mov_b32 v33, v32\n"
    "s_nop 1\n"
    "v_permlane32_swap_b32 v32, v33\n"
    "v_pk_fma_f32 v[112:113], v[112:113], s[50:51], v[32:33] op_sel_hi:[1,1,0] neg_lo:[0,0,1] neg_hi:[0,0,1]\n"
    "v_pk_fma_f32 v[114:115], v[114:115], s[50:51], v[32:33] op_sel_hi:[1,1,0] neg_lo:[0,0,1] neg_hi:[0,0,1]\n"
    "v_pk_fma_f32 v[116:117], v[116:117], s[50:51], v[32:33] op_sel_hi:[1,1,0] neg_lo:[0,0,1] neg_hi:[0,0,1]\n"
    "v_pk_fma_f32 v[118:119], v[118:119], s[50:51], v[32:33] op_sel_hi:[1,1,0] neg_lo:[0,0,1] neg_hi:[0,0,1]\n"
    "v_pk_fma_f32 v[120:121], v[120:121], s[50:51], v[32:33] op_sel:[0,0,1] op_sel_hi:[1,1,1] neg_lo:[0,0,1] neg_hi:[0,0,1]\n"
    "v_pk_fma_f32 v[122:123], v[122:123], s[50:51], v[32:33] op_sel:[0,0,1] op_sel_hi:[1,1,1] neg_lo:[0,0,1] neg_hi:[0,0,1]\n"
    "v_pk_fma_f32 v[124:125], v[124:125], s[50:51], v[32:33] op_sel:[0,0,1] op_sel_hi:[1,1,1] neg_lo:[0,0,1] neg_hi:[0,0,1]\n"
    "v_pk_fma_f32 v[126:127], v[126:127], s[50:51], v[32:33] op_sel:[0,0,1] op_sel_hi:[1,1,1] neg_lo:[0,0,1] neg_hi:[0,0,1]\n"
    "v_exp_f32 v112, v112\n"
    "v_exp_f32 v113, v113\n"
    "v_exp_f32 v114, v114\n"
    "v_exp_f32 v115, v115\n"
    "v_exp_f32 v116, v116\n"
    "v_exp_f32 v117, v117\n"
    "v_exp_f32 v118, v118\n"
    "v_exp_f32 v119, v119\n"
    "v_exp_f32 v120, v120\n"
    "v_exp_f32 v121, v121\n"
    "v_exp_f32 v122, v122\n"
    "v_exp_f32 v123, v123\n"
    "v_exp_f32 v124, v124\n"
    "v_exp_f32 v125, v125\n"
    "v_exp_f32 v126, v126\n"
    "v_exp_f32 v127, v127\n"
    "v_pk_add_f32 v[36:37], v[112:113], v[114:115]\n"
    "v_pk_add_f32 v[38:39], v[116:117], v[118:119]\n"
    "v_pk_add_f32 v[40:41], v[120:121], v[122:123]\n"
    "v_pk_add_f32 v[42:43], v[124:125], v[126:127]\n"
    "v_pk_add_f32 v[36:37], v[36:37], v[38:39]\n"
    "v_pk_add_f32 v[40:41], v[40:41], v[42:43]\n"
    "v_add_f32 v34, v36, v37\n"
    "v_add_f32 v35, v40, v41\n"
    "s_nop 1\n"
    "v_permlane32_swap_b32 v34, v35\n"
    "v_add_f32 v34, v34, v35\n"
    "v_mov_b32 v35, v34\n"
    "s_nop 1\n"
    "v_permlane16_swap_b32 v34, v35\n"
    "v_add_f32 v34, v34, v35\n"
    "v_rcp_f32 v34, v34\n"
    "s_nop 0\n"
    "v_mov_b32 v35, v34\n"
    "s_nop 1\n"
    "v_permlane32_swap_b32 v34, v35\n"
    "v_pk_mul_f32 v[112:113], v[112:113], v[34:35] op_sel_hi:[1,0]\n"
    "v_pk_mul_f32 v[114:115], v[114:115], v[34:35] op_sel_hi:[1,0]\n"
    "v_pk_mul_f32 v[116:117], v[116:117], v[34:35] op_sel_hi:[1,0]\n"
    "v_pk_mul_f32 v[118:119], v[118:119], v[34:35] op_sel_hi:[1,0]\n"
    "v_pk_mul_f32 v[120:121], v[120:121], v[34:35] op_sel:[0,1] op_sel_hi:[1,1]\n"
    "v_pk_mul_f32 v[122:123], v[122:123], v[34:35] op_sel:[0,1] op_sel_hi:[1,1]\n"
    "v_cvt_pk_bf16_f32 v40, abs(v112), abs(v113)\n"
    "v_cvt_pk_bf16_f32 v41, abs(v114), abs(v115)\n"
    "v_cvt_pk_bf16_f32 v42, abs(v116), abs(v117)\n"
    "v_cvt_pk_bf16_f32 v43, abs(v118), abs(v119)\n"
    "buffer_store_dwordx4 v[40:43], v2, s[28:31], s48 offen sc1\n"
    "v_pk_mul_f32 v[124:125], v[124:125], v[34:35] op_sel:[0,1] op_sel_hi:[1,1]\n"
    "v_pk_mul_f32 v[126:127], v[126:127], v[34:35] op_sel:[0,1] op_sel_hi:[1,1]\n"
    "v_cvt_pk_bf16_f32 v44, abs(v120), abs(v121)\n"
    "v_cvt_pk_bf16_f32 v45, abs(v122), abs(v123)\n"
    "v_cvt_pk_bf16_f32 v46, abs(v124), abs(v125)\n"
    "v_cvt_pk_bf16_f32 v47, abs(v126), abs(v127)\n"
    "buffer_store_dwordx4 v[44:47], v2, s[28:31], s48 offen offset:1024 sc1\n"
    "s_buffer_load_dwordx2 s[96:97], s[28:31], s56 offset:24\n"
    "s_setprio 0\n"
    "s_barrier\n"
    "s_waitcnt lgkmcnt(0)\n"
    "s_add_u32 s96, s96, 0x7f4a7c15\n"
    "s_addc_u32 s97, s97, 0x9e3779b9\n"
    "v_mov_b32 v14, 0x20000\n"
    "v_mov_b32 v15, 0\n"
    "ds_write_b32 v14, v15\n"
    "s_waitcnt lgkmcnt(0)\n"
    "s_barrier\n"
    "s_mov_b32 s42, 0\n"
    ".Lmainred:\n"
    ".Lpad_1a_1:\n"
    "s_cbranch_execz .Lpadret_1a\n"
    ".Lpad_2a_0:\n"
    "s_cbranch_execz .Lpad_2a_1\n"
    "s_lshr_b32 s52, s37, 1\n"
    "s_lshl2_add_u32 s52, s39, s52\n"
    "s_and_b32 s50, s37, 1\n"
    "s_lshl1_add_u32 s50, s50, s42\n"
    "s_lshl2_add_u32 s50, s38, s50\n"
    "s_lshl_b32 s53, s50, 5\n"
    "s_lshl_b32 s46, s2, 14\n"
    "s_add_u32 s46, s46, 2097152\n"
    "s_mov_b32 s51, s41\n"
    "s_lshl_b32 s56, s2, 6\n"
    "s_lshl1_add_u32 s40, s56, 163904\n"
    "s_add_u32 s56, s56, 24\n"
    "s_mov_b32 s55, 1\n"
    ".Lreduce:\n"
    "s_lshl_b32 s47, s51, 9\n"
    "s_add_u32 s46, s46, s47\n"
    "s_add_u32 s47, s46, 4096\n"
    "s_and_b32 s42, s51, 1\n"
    "s_lshl3_add_u32 s53, s42, s53\n"
    "s_lshr_b32 s42, s51, 1\n"
    "s_lshl1_add_u32 s52, s52, s42\n"
    "s_mul_i32 s52, s52, 18432\n"
    "s_lshl1_add_u32 s52, s53, s52\n"
    "v_and_b32 v10, 31, v0\n"
    "v_lshrrev_b32 v11, 5, v0\n"
    "v_lshlrev_b32 v12, 4, v10\n"
    "v_lshl_or_b32 v12, v11, 13, v12\n"
    "v_and_b32 v10, 15, v0\n"
    "v_mul_u32_u24 v10, 1152, v10\n"
    "v_lshl_add_u32 v10, v11, 5, v10\n"
    "v_lshrrev_b32 v11, 1, v0\n"
    "v_and_or_b32 v10, v11, 8, v10\n"
    "s_mov_b32 s54, 0xffff0000\n"
    "s_bitcmp1_b32 s2, 3\n"
    "s_cbranch_scc1 .Lr2p\n"
    ".Lred_nobar:\n"
    "s_lshl_b32 s45, s55, 24\n"
    "s_add_u32 s45, s45, 524288\n"
    "s_movk_i32 s48, 0xff\n"
    ".Lrp:\n"
    "s_mov_b64 exec, 0xff\n"
    "s_cmp_eq_u32 s55, 0\n"
    "s_cbranch_scc1 .Lrp_own\n"
    ".Lrp_l:\n"
    "ds_read_b32 v15, v14\n"
    "s_sub_u32 s45, s45, 1\n"
    "s_cbranch_scc1 .Lrp_done\n"
    "s_waitcnt lgkmcnt(0)\n"
    "v_readfirstlane_b32 s49, v15\n"
    "s_nop 0\n"
    "s_and_b32 s49, s49, s48\n"
    "s_cbranch_scc0 .Lrp_l\n"
    "s_branch .Lrp_new\n"
    ".Lrp_own:\n"
    "v_mov_b64 v[22:23], 0\n"
    "buffer_atomic_add_x2 v[22:23], v3, s[28:31], s40 offen sc0 sc1\n"
    "s_sub_u32 s45, s45, 1\n"
    "s_cbranch_scc1 .Lrp_done\n"
    "s_waitcnt vmcnt(0)\n"
    "v_cmp_eq_u64 vcc, s[96:97], v[22:23]\n"
    "s_nop 1\n"
    "s_and_b32 s49, vcc_lo, s48\n"
    "s_cbranch_scc0 .Lrp\n"
    ".Lrp_new:\n"
    "s_bfe_i32 exec_lo, s49, 0x10000\n"
    "s_bfe_i32 exec_hi, s49, 0x10004\n"
    "s_cbranch_execz .Lrp_s0\n"
    "buffer_load_dwordx4 v[32:35], v12, s[28:31], s46 offen offset:0 sc1\n"
    ".Lrp_s0:\n"
    "s_bfe_i32 exec_lo, s49, 0x10001\n"
    "s_bfe_i32 exec_hi, s49, 0x10005\n"
    "s_cbranch_execz .Lrp_s1\n"
    "buffer_load_dwordx4 v[36:39], v12, s[28:31], s46 offen offset:2048 sc1\n"
    ".Lrp_s1:\n"
    "s_bfe_i32 exec_lo, s49, 0x10002\n"
    "s_bfe_i32 exec_hi, s49, 0x10006\n"
    "s_cbranch_execz .Lrp_s2\n"
    "buffer_load_dwordx4 v[40:43], v12, s[28:31], s47 offen offset:0 sc1\n"
    ".Lrp_s2:\n"
    "s_bfe_i32 exec_lo, s49, 0x10003\n"
    "s_bfe_i32 exec_hi, s49, 0x10007\n"
    "s_cbranch_execz .Lrp_s3\n"
    "buffer_load_dwordx4 v[44:47], v12, s[28:31], s47 offen offset:2048 sc1\n"
    ".Lrp_s3:\n"
    "s_andn2_b32 s48, s48, s49\n"
    "s_cbranch_scc1 .Lrp\n"
    ".Lrp_done:\n"
    "s_mov_b64 exec, 1\n"
    "v_mov_b64 v[4:5], s[96:97]\n"
    "buffer_store_dwordx2 v[4:5], off, s[28:31], s56 offset:0 sc1\n"
    "s_mov_b64 exec, -1\n"
    "s_waitcnt vmcnt(1)\n"
    "v_lshlrev_b32 v56, 16, v32\n"
    "v_and_b32 v57, s54, v32\n"
    "v_lshlrev_b32 v58, 16, v33\n"
    "v_and_b32 v59, s54, v33\n"
    "v_lshlrev_b32 v60, 16, v34\n"
    "v_and_b32 v61, s54, v34\n"
    "v_lshlrev_b32 v62, 16, v35\n"
    "v_and_b32 v63, s54, v35\n"
    "v_lshlrev_b32 v48, 16, v36\n"
    "v_and_b32 v49, s54, v36\n"
    "v_lshlrev_b32 v50, 16, v37\n"
    "v_and_b32 v51, s54, v37\n"
    "v_lshlrev_b32 v52, 16, v38\n"
    "v_and_b32 v53, s54, v38\n"
    "v_lshlrev_b32 v54, 16, v39\n"
    "v_and_b32 v55, s54, v39\n"
    "v_pk_add_f32 v[56:57], v[56:57], v[48:49]\n"
    "v_pk_add_f32 v[58:59], v[58:59], v[50:51]\n"
    "v_pk_add_f32 v[60:61], v[60:61], v[52:53]\n"
    "v_pk_add_f32 v[62:63], v[62:63], v[54:55]\n"
    "v_lshlrev_b32 v48, 16, v40\n"
    "v_and_b32 v49, s54, v40\n"
    "v_lshlrev_b32 v50, 16, v41\n"
    "v_and_b32 v51, s54, v41\n"
    "v_lshlrev_b32 v52, 16, v42\n"
    "v_and_b32 v53, s54, v42\n"
    "v_lshlrev_b32 v54, 16, v43\n"
    "v_and_b32 v55, s54, v43\n"
    "v_pk_add_f32 v[56:57], v[56:57], v[48:49]\n"
    "v_pk_add_f32 v[58:59], v[58:59], v[50:51]\n"
    "v_pk_add_f32 v[60:61], v[60:61], v[52:53]\n"
    "v_pk_add_f32 v[62:63], v[62:63], v[54:55]\n"
    "v_lshlrev_b32 v48, 16, v44\n"
    "v_and_b32 v49, s54, v44\n"
    "v_lshlrev_b32 v50, 16, v45\n"
    "v_and_b32 v51, s54, v45\n"
    "v_lshlrev_b32 v52, 16, v46\n"
    "v_and_b32 v53, s54, v46\n"
    "v_lshlrev_b32 v54, 16, v47\n"
    "v_and_b32 v55, s54, v47\n"
    "v_pk_add_f32 v[56:57], v[56:57], v[48:49]\n"
    "v_pk_add_f32 v[58:59], v[58:59], v[50:51]\n"
    "v_pk_add_f32 v[60:61], v[60:61], v[52:53]\n"
    "v_pk_add_f32 v[62:63], v[62:63], v[54:55]\n"
    "s_nop 1\n"
    "v_permlane32_swap_b32 v56, v60\n"
    "v_permlane32_swap_b32 v57, v61\n"
    "v_permlane32_swap_b32 v58, v62\n"
    "v_permlane32_swap_b32 v59, v63\n"
    "v_pk_add_f32 v[56:57], v[56:57], v[60:61]\n"
    "v_pk_add_f32 v[58:59], v[58:59], v[62:63]\n"
    "v_cvt_pk_bf16_f32 v48, v56, v57\n"
    "v_cvt_pk_bf16_f32 v49, v58, v59\n"
    "buffer_store_dwordx2 v[48:49], v10, s[32:35], s52 offen\n"
    ".Lend:\n"
    "s_endpgm\n"
    ".Lpad_1d_0:\n"
    "s_cbranch_execz .Lpadret_1d\n"
    ".Lr2m:\n"
    "s_and_b32 s44, s41, 1\n"
    "s_lshr_b32 s45, s41, 1\n"
    "s_lshl_b32 s42, s44, 7\n"
    "v_xor_b32 v10, s42, v10\n"
    "v_xor_b32 v11, 32, v10\n"
    "v_xor_b32 v12, 64, v10\n"
    "v_xor_b32 v13, 0x60, v10\n"
    "v_xor_b32 v24, v22, v23\n"
    "v_lshlrev_b32 v24, 4, v24\n"
    "v_lshl_or_b32 v4, v23, 8, v24\n"
    "s_lshl_b32 s42, s45, 14\n"
    "v_add_u32 v19, s42, v2\n"
    "s_add_u32 s42, s42, 0x18000\n"
    "v_add_u32 v4, s42, v4\n"
    "v_xor_b32 v5, 64, v4\n"
    "v_xor_b32 v6, 0x80, v4\n"
    "v_xor_b32 v7, 0xc0, v4\n"
    "s_lshl_b32 s42, s43, 4\n"
    "s_lshl2_add_u32 s42, s45, s42\n"
    "s_add_u32 s42, s42, s44\n"
    "s_lshl3_add_u32 s50, s42, s37\n"
    "s_lshl_b32 s54, s50, 11\n"
    "s_add_u32 s54, s54, 2097152\n"
    "s_add_u32 s55, s54, 0x20000\n"
    "s_add_u32 s56, s54, 0x8000\n"
    "s_add_u32 s57, s54, 0x28000\n"
    "s_lshl_b32 s48, s42, 6\n"
    "s_lshl2_add_u32 s49, s37, s48\n"
    "s_lshl1_add_u32 s49, s49, 163904\n"
    "s_waitcnt lgkmcnt(0)\n"
    "s_buffer_load_dwordx2 s[96:97], s[28:31], s48 offset:24\n"
    "s_buffer_load_dwordx2 s[98:99], s[28:31], s48 offset:536\n"
    "s_buffer_load_dwordx2 s[58:59], s[28:31], s48 offset:152\n"
    "s_buffer_load_dwordx2 s[4:5], s[28:31], s48 offset:664\n"
    "s_lshl_b32 s40, s2, 6\n"
    "s_buffer_load_dwordx2 s[6:7], s[28:31], s40 offset:24\n"
    "s_waitcnt lgkmcnt(0)\n"
    "s_add_u32 s96, s96, 0x7f4a7c15\n"
    "s_addc_u32 s97, s97, 0x9e3779b9\n"
    "s_add_u32 s98, s98, 0x7f4a7c15\n"
    "s_addc_u32 s99, s99, 0x9e3779b9\n"
    "s_add_u32 s58, s58, 0x7f4a7c15\n"
    "s_addc_u32 s59, s59, 0x9e3779b9\n"
    "s_add_u32 s4, s4, 0x7f4a7c15\n"
    "s_addc_u32 s5, s5, 0x9e3779b9\n"
    "s_add_u32 s6, s6, 0x7f4a7c15\n"
    "s_addc_u32 s7, s7, 0x9e3779b9\n"
    "s_lshl1_add_u32 s40, s40, 163904\n"
    "s_mov_b64 exec, 0\n"
    "s_cmp_eq_u32 s41, 0\n"
    "s_cbranch_scc1 .Lpad_2a_0\n"
    "s_cmp_eq_u32 s41, 1\n"
    "s_cbranch_scc1 .Lpad_2b_0\n"
    "s_cmp_eq_u32 s41, 2\n"
    "s_cbranch_scc1 .Lpad_2c_0\n"
    "s_branch .Lpad_2d_0\n"
    ".Lpadret_2a:\n"
    ".Lpadret_2b:\n"
    ".Lpadret_2c:\n"
    ".Lpadret_2d:\n"
    "s_mov_b64 exec, -1\n"
    "s_barrier\n"
    "ds_read_b64_tr_b16 v[14:15], v10 offset:0\n"
    "ds_read_b64_tr_b16 v[16:17], v10 offset:1024\n"
    "ds_read_b128 v[32:35], v4 offset:0\n"
    "ds_read_b128 v[36:39], v4 offset:4096\n"
    "ds_read_b128 v[40:43], v4 offset:8192\n"
    "ds_read_b128 v[44:47], v4 offset:12288\n"
    "ds_read_b64_tr_b16 v[20:21], v10 offset:8192\n"
    "ds_read_b64_tr_b16 v[22:23], v10 offset:9216\n"
    "ds_read_b128 v[48:51], v5 offset:0\n"
    "ds_read_b128 v[52:55], v5 offset:4096\n"
    "ds_read_b128 v[56:59], v5 offset:8192\n"
    "ds_read_b128 v[60:63], v5 offset:12288\n"
    "s_waitcnt lgkmcnt(10)\n"
    "s_waitcnt lgkmcnt(9)\n"
    "v_mfma_f32_16x16x32_bf16 v[96:99], v[32:35], v[14:17], 0\n"
    "ds_read_b64_tr_b16 v[24:25], v10 offset:16384\n"
    "ds_read_b128 v[64:67], v6 offset:0\n"
    "s_waitcnt lgkmcnt(10)\n"
    "v_mfma_f32_16x16x32_bf16 v[100:103], v[36:39], v[14:17], 0\n"
    "ds_read_b64_tr_b16 v[26:27], v10 offset:17408\n"
    "ds_read_b128 v[68:71], v6 offset:4096\n"
    "s_waitcnt lgkmcnt(11)\n"
    "v_mfma_f32_16x16x32_bf16 v[104:107], v[40:43], v[14:17], 0\n"
    "ds_read_b128 v[72:75], v6 offset:8192\n"
    "s_waitcnt lgkmcnt(11)\n"
    "v_mfma_f32_16x16x32_bf16 v[108:111], v[44:47], v[14:17], 0\n"
    "ds_read_b128 v[76:79], v6 offset:12288\n"
    "s_waitcnt lgkmcnt(6)\n"
    "v_mfma_f32_16x16x32_bf16 v[96:99], v[48:51], v[20:23], v[96:99]\n"
    "ds_read_b64_tr_b16 v[28:29], v10 offset:24576\n"
    "ds_read_b128 v[80:83], v7 offset:0\n"
    "v_mfma_f32_16x16x32_bf16 v[100:103], v[52:55], v[20:23], v[100:103]\n"
    "ds_read_b64_tr_b16 v[30:31], v10 offset:25600\n"
    "ds_read_b128 v[84:87], v7 offset:4096\n"
    "v_mfma_f32_16x16x32_bf16 v[104:107], v[56:59], v[20:23], v[104:107]\n"
    "ds_read_b128 v[88:91], v7 offset:8192\n"
    "v_mfma_f32_16x16x32_bf16 v[108:111], v[60:63], v[20:23], v[108:111]\n"
    "ds_read_b128 v[92:95], v7 offset:12288\n"
    "s_waitcnt lgkmcnt(6)\n"
    "v_mfma_f32_16x16x32_bf16 v[96:99], v[64:67], v[24:27], v[96:99]\n"
    "ds_read_b64_tr_b16 v[14:15], v11 offset:0\n"
    "v_mfma_f32_16x16x32_bf16 v[100:103], v[68:71], v[24:27], v[100:103]\n"
    "ds_read_b64_tr_b16 v[16:17], v11 offset:1024\n"
    "v_mfma_f32_16x16x32_bf16 v[104:107], v[72:75], v[24:27], v[104:107]\n"
    "v_mfma_f32_16x16x32_bf16 v[108:111], v[76:79], v[24:27], v[108:111]\n"
    "s_waitcnt lgkmcnt(2)\n"
    "v_mfma_f32_16x16x32_bf16 v[96:99], v[80:83], v[28:31], v[96:99]\n"
    "ds_read_b64_tr_b16 v[20:21], v11 offset:8192\n"
    "v_mfma_f32_16x16x32_bf16 v[100:103], v[84:87], v[28:31], v[100:103]\n"
    "ds_read_b64_tr_b16 v[22:23], v11 offset:9216\n"
    "v_mfma_f32_16x16x32_bf16 v[104:107], v[88:91], v[28:31], v[104:107]\n"
    "v_mfma_f32_16x16x32_bf16 v[108:111], v[92:95], v[28:31], v[108:111]\n"
    "s_waitcnt lgkmcnt(2)\n"
    "v_mfma_f32_16x16x32_bf16 v[112:115], v[32:35], v[14:17], 0\n"
    "ds_read_b64_tr_b16 v[24:25], v11 offset:16384\n"
    "v_mfma_f32_16x16x32_bf16 v[116:119], v[36:39], v[14:17], 0\n"
    "ds_read_b64_tr_b16 v[26:27], v11 offset:17408\n"
    "v_mfma_f32_16x16x32_bf16 v[120:123], v[40:43], v[14:17], 0\n"
    "v_mfma_f32_16x16x32_bf16 v[124:127], v[44:47], v[14:17], 0\n"
    "s_waitcnt lgkmcnt(2)\n"
    "v_mfma_f32_16x16x32_bf16 v[112:115], v[48:51], v[20:23], v[112:115]\n"
    "ds_read_b64_tr_b16 v[28:29], v11 offset:24576\n"
    "v_mfma_f32_16x16x32_bf16 v[116:119], v[52:55], v[20:23], v[116:119]\n"
    "ds_read_b64_tr_b16 v[30:31], v11 offset:25600\n"
    "v_mfma_f32_16x16x32_bf16 v[120:123], v[56:59], v[20:23], v[120:123]\n"
    "v_cvt_pk_bf16_f32 v128, v96, v97\n"
    "v_mfma_f32_16x16x32_bf16 v[124:127], v[60:63], v[20:23], v[124:127]\n"
    "v_cvt_pk_bf16_f32 v129, v98, v99\n"
    "s_waitcnt lgkmcnt(2)\n"
    "v_mfma_f32_16x16x32_bf16 v[112:115], v[64:67], v[24:27], v[112:115]\n"
    "ds_read_b64_tr_b16 v[14:15], v12 offset:0\n"
    "v_mfma_f32_16x16x32_bf16 v[116:119], v[68:71], v[24:27], v[116:119]\n"
    "ds_read_b64_tr_b16 v[16:17], v12 offset:1024\n"
    "v_mfma_f32_16x16x32_bf16 v[120:123], v[72:75], v[24:27], v[120:123]\n"
    "v_cvt_pk_bf16_f32 v130, v100, v101\n"
    "v_mfma_f32_16x16x32_bf16 v[124:127], v[76:79], v[24:27], v[124:127]\n"
    "v_cvt_pk_bf16_f32 v131, v102, v103\n"
    "s_waitcnt lgkmcnt(2)\n"
    "v_mfma_f32_16x16x32_bf16 v[112:115], v[80:83], v[28:31], v[112:115]\n"
    "ds_read_b64_tr_b16 v[20:21], v12 offset:8192\n"
    "v_cvt_pk_bf16_f32 v192, v104, v105\n"
    "v_mfma_f32_16x16x32_bf16 v[116:119], v[84:87], v[28:31], v[116:119]\n"
    "ds_read_b64_tr_b16 v[22:23], v12 offset:9216\n"
    "v_cvt_pk_bf16_f32 v193, v106, v107\n"
    "v_mfma_f32_16x16x32_bf16 v[120:123], v[88:91], v[28:31], v[120:123]\n"
    "v_cvt_pk_bf16_f32 v194, v108, v109\n"
    "v_mfma_f32_16x16x32_bf16 v[124:127], v[92:95], v[28:31], v[124:127]\n"
    "v_cvt_pk_bf16_f32 v195, v110, v111\n"
    "s_waitcnt lgkmcnt(2)\n"
    "v_mfma_f32_16x16x32_bf16 v[96:99], v[32:35], v[14:17], 0\n"
    "ds_read_b64_tr_b16 v[24:25], v12 offset:16384\n"
    "v_mfma_f32_16x16x32_bf16 v[100:103], v[36:39], v[14:17], 0\n"
    "ds_read_b64_tr_b16 v[26:27], v12 offset:17408\n"
    "v_mfma_f32_16x16x32_bf16 v[104:107], v[40:43], v[14:17], 0\n"
    "v_mfma_f32_16x16x32_bf16 v[108:111], v[44:47], v[14:17], 0\n"
    "s_waitcnt lgkmcnt(2)\n"
    "v_mfma_f32_16x16x32_bf16 v[96:99], v[48:51], v[20:23], v[96:99]\n"
    "ds_read_b64_tr_b16 v[28:29], v12 offset:24576\n"
    "v_mfma_f32_16x16x32_bf16 v[100:103], v[52:55], v[20:23], v[100:103]\n"
    "ds_read_b64_tr_b16 v[30:31], v12 offset:25600\n"
    "v_mfma_f32_16x16x32_bf16 v[104:107], v[56:59], v[20:23], v[104:107]\n"
    "v_cvt_pk_bf16_f32 v132, v112, v113\n"
    "v_mfma_f32_16x16x32_bf16 v[108:111], v[60:63], v[20:23], v[108:111]\n"
    "v_cvt_pk_bf16_f32 v133, v114, v115\n"
    "s_waitcnt lgkmcnt(2)\n"
    "v_mfma_f32_16x16x32_bf16 v[96:99], v[64:67], v[24:27], v[96:99]\n"
    "ds_read_b64_tr_b16 v[14:15], v13 offset:0\n"
    "v_mfma_f32_16x16x32_bf16 v[100:103], v[68:71], v[24:27], v[100:103]\n"
    "ds_read_b64_tr_b16 v[16:17], v13 offset:1024\n"
    "v_mfma_f32_16x16x32_bf16 v[104:107], v[72:75], v[24:27], v[104:107]\n"
    "v_cvt_pk_bf16_f32 v134, v116, v117\n"
    "v_mfma_f32_16x16x32_bf16 v[108:111], v[76:79], v[24:27], v[108:111]\n"
    "v_cvt_pk_bf16_f32 v135, v118, v119\n"
    "s_waitcnt lgkmcnt(2)\n"
    "v_mfma_f32_16x16x32_bf16 v[96:99], v[80:83], v[28:31], v[96:99]\n"
    "ds_read_b64_tr_b16 v[20:21], v13 offset:8192\n"
    "v_cvt_pk_bf16_f32 v196, v120, v121\n"
    "v_mfma_f32_16x16x32_bf16 v[100:103], v[84:87], v[28:31], v[100:103]\n"
    "ds_read_b64_tr_b16 v[22:23], v13 offset:9216\n"
    "v_cvt_pk_bf16_f32 v197, v122, v123\n"
    "v_mfma_f32_16x16x32_bf16 v[104:107], v[88:91], v[28:31], v[104:107]\n"
    "v_cvt_pk_bf16_f32 v198, v124, v125\n"
    "v_mfma_f32_16x16x32_bf16 v[108:111], v[92:95], v[28:31], v[108:111]\n"
    "v_cvt_pk_bf16_f32 v199, v126, v127\n"
    ".Lpad_2b_1:\n"
    "s_cbranch_execz .Lpad_2b_2\n"
    "s_waitcnt lgkmcnt(2)\n"
    "v_mfma_f32_16x16x32_bf16 v[112:115], v[32:35], v[14:17], 0\n"
    "ds_read_b64_tr_b16 v[24:25], v13 offset:16384\n"
    "v_mfma_f32_16x16x32_bf16 v[116:119], v[36:39], v[14:17], 0\n"
    "ds_read_b64_tr_b16 v[26:27], v13 offset:17408\n"
    "v_mfma_f32_16x16x32_bf16 v[120:123], v[40:43], v[14:17], 0\n"
    "v_mfma_f32_16x16x32_bf16 v[124:127], v[44:47], v[14:17], 0\n"
    "s_waitcnt lgkmcnt(2)\n"
    "v_mfma_f32_16x16x32_bf16 v[112:115], v[48:51], v[20:23], v[112:115]\n"
    "ds_read_b64_tr_b16 v[28:29], v13 offset:24576\n"
    "v_mfma_f32_16x16x32_bf16 v[116:119], v[52:55], v[20:23], v[116:119]\n"
    "ds_read_b64_tr_b16 v[30:31], v13 offset:25600\n"
    "v_mfma_f32_16x16x32_bf16 v[120:123], v[56:59], v[20:23], v[120:123]\n"
    "v_cvt_pk_bf16_f32 v160, v96, v97\n"
    "v_mfma_f32_16x16x32_bf16 v[124:127], v[60:63], v[20:23], v[124:127]\n"
    "v_cvt_pk_bf16_f32 v161, v98, v99\n"
    "s_waitcnt lgkmcnt(2)\n"
    "v_mfma_f32_16x16x32_bf16 v[112:115], v[64:67], v[24:27], v[112:115]\n"
    "s_barrier\n"
    "ds_read_b64_tr_b16 v[14:15], v10 offset:32768\n"
    "v_mfma_f32_16x16x32_bf16 v[116:119], v[68:71], v[24:27], v[116:119]\n"
    "ds_read_b64_tr_b16 v[16:17], v10 offset:33792\n"
    "v_mfma_f32_16x16x32_bf16 v[120:123], v[72:75], v[24:27], v[120:123]\n"
    "v_cvt_pk_bf16_f32 v162, v100, v101\n"
    "v_mfma_f32_16x16x32_bf16 v[124:127], v[76:79], v[24:27], v[124:127]\n"
    "v_cvt_pk_bf16_f32 v163, v102, v103\n"
    "s_waitcnt lgkmcnt(2)\n"
    "v_mfma_f32_16x16x32_bf16 v[112:115], v[80:83], v[28:31], v[112:115]\n"
    "ds_read_b64_tr_b16 v[20:21], v10 offset:40960\n"
    "v_cvt_pk_bf16_f32 v224, v104, v105\n"
    "v_mfma_f32_16x16x32_bf16 v[116:119], v[84:87], v[28:31], v[116:119]\n"
    "ds_read_b64_tr_b16 v[22:23], v10 offset:41984\n"
    "v_cvt_pk_bf16_f32 v225, v106, v107\n"
    "v_mfma_f32_16x16x32_bf16 v[120:123], v[88:91], v[28:31], v[120:123]\n"
    "v_cvt_pk_bf16_f32 v226, v108, v109\n"
    "v_mfma_f32_16x16x32_bf16 v[124:127], v[92:95], v[28:31], v[124:127]\n"
    "v_cvt_pk_bf16_f32 v227, v110, v111\n"
    "s_waitcnt lgkmcnt(2)\n"
    "v_mfma_f32_16x16x32_bf16 v[96:99], v[32:35], v[14:17], 0\n"
    "ds_read_b64_tr_b16 v[24:25], v10 offset:49152\n"
    "v_mfma_f32_16x16x32_bf16 v[100:103], v[36:39], v[14:17], 0\n"
    "ds_read_b64_tr_b16 v[26:27], v10 offset:50176\n"
    "v_mfma_f32_16x16x32_bf16 v[104:107], v[40:43], v[14:17], 0\n"
    "v_mfma_f32_16x16x32_bf16 v[108:111], v[44:47], v[14:17], 0\n"
    "s_waitcnt lgkmcnt(2)\n"
    "v_mfma_f32_16x16x32_bf16 v[96:99], v[48:51], v[20:23], v[96:99]\n"
    "ds_read_b64_tr_b16 v[28:29], v10 offset:57344\n"
    "v_mfma_f32_16x16x32_bf16 v[100:103], v[52:55], v[20:23], v[100:103]\n"
    "ds_read_b64_tr_b16 v[30:31], v10 offset:58368\n"
    "v_mfma_f32_16x16x32_bf16 v[104:107], v[56:59], v[20:23], v[104:107]\n"
    "v_cvt_pk_bf16_f32 v164, v112, v113\n"
    "v_mfma_f32_16x16x32_bf16 v[108:111], v[60:63], v[20:23], v[108:111]\n"
    "v_cvt_pk_bf16_f32 v165, v114, v115\n"
    "s_waitcnt lgkmcnt(2)\n"
    "v_mfma_f32_16x16x32_bf16 v[96:99], v[64:67], v[24:27], v[96:99]\n"
    "ds_read_b64_tr_b16 v[14:15], v11 offset:32768\n"
    "v_mfma_f32_16x16x32_bf16 v[100:103], v[68:71], v[24:27], v[100:103]\n"
    "ds_read_b64_tr_b16 v[16:17], v11 offset:33792\n"
    "v_mfma_f32_16x16x32_bf16 v[104:107], v[72:75], v[24:27], v[104:107]\n"
    "v_cvt_pk_bf16_f32 v166, v116, v117\n"
    "v_mfma_f32_16x16x32_bf16 v[108:111], v[76:79], v[24:27], v[108:111]\n"
    "v_cvt_pk_bf16_f32 v167, v118, v119\n"
    "s_waitcnt lgkmcnt(2)\n"
    "v_mfma_f32_16x16x32_bf16 v[96:99], v[80:83], v[28:31], v[96:99]\n"
    "ds_read_b64_tr_b16 v[20:21], v11 offset:40960\n"
    "v_cvt_pk_bf16_f32 v228, v120, v121\n"
    "v_mfma_f32_16x16x32_bf16 v[100:103], v[84:87], v[28:31], v[100:103]\n"
    "ds_read_b64_tr_b16 v[22:23], v11 offset:41984\n"
    "v_cvt_pk_bf16_f32 v229, v122, v123\n"
    "v_mfma_f32_16x16x32_bf16 v[104:107], v[88:91], v[28:31], v[104:107]\n"
    "v_cvt_pk_bf16_f32 v230, v124, v125\n"
    "v_mfma_f32_16x16x32_bf16 v[108:111], v[92:95], v[28:31], v[108:111]\n"
    "v_cvt_pk_bf16_f32 v231, v126, v127\n"
    "s_waitcnt lgkmcnt(2)\n"
    "v_mfma_f32_16x16x32_bf16 v[112:115], v[32:35], v[14:17], 0\n"
    "ds_read_b64_tr_b16 v[24:25], v11 offset:49152\n"
    "v_mfma_f32_16x16x32_bf16 v[116:119], v[36:39], v[14:17], 0\n"
    "ds_read_b64_tr_b16 v[26:27], v11 offset:50176\n"
    "v_mfma_f32_16x16x32_bf16 v[120:123], v[40:43], v[14:17], 0\n"
    "v_mfma_f32_16x16x32_bf16 v[124:127], v[44:47], v[14:17], 0\n"
    "s_waitcnt lgkmcnt(2)\n"
    "v_mfma_f32_16x16x32_bf16 v[112:115], v[48:51], v[20:23], v[112:115]\n"
    "ds_read_b64_tr_b16 v[28:29], v11 offset:57344\n"
    "v_mfma_f32_16x16x32_bf16 v[116:119], v[52:55], v[20:23], v[116:119]\n"
    "ds_read_b64_tr_b16 v[30:31], v11 offset:58368\n"
    "v_mfma_f32_16x16x32_bf16 v[120:123], v[56:59], v[20:23], v[120:123]\n"
    "v_cvt_pk_bf16_f32 v136, v96, v97\n"
    "v_mfma_f32_16x16x32_bf16 v[124:127], v[60:63], v[20:23], v[124:127]\n"
    "v_cvt_pk_bf16_f32 v137, v98, v99\n"
    "s_waitcnt lgkmcnt(2)\n"
    "v_mfma_f32_16x16x32_bf16 v[112:115], v[64:67], v[24:27], v[112:115]\n"
    "ds_read_b64_tr_b16 v[14:15], v12 offset:32768\n"
    "v_mfma_f32_16x16x32_bf16 v[116:119], v[68:71], v[24:27], v[116:119]\n"
    "ds_read_b64_tr_b16 v[16:17], v12 offset:33792\n"
    "v_mfma_f32_16x16x32_bf16 v[120:123], v[72:75], v[24:27], v[120:123]\n"
    "v_cvt_pk_bf16_f32 v138, v100, v101\n"
    "v_mfma_f32_16x16x32_bf16 v[124:127], v[76:79], v[24:27], v[124:127]\n"
    "v_cvt_pk_bf16_f32 v139, v102, v103\n"
    "s_waitcnt lgkmcnt(2)\n"
    "v_mfma_f32_16x16x32_bf16 v[112:115], v[80:83], v[28:31], v[112:115]\n"
    "ds_read_b64_tr_b16 v[20:21], v12 offset:40960\n"
    "v_cvt_pk_bf16_f32 v200, v104, v105\n"
    "v_mfma_f32_16x16x32_bf16 v[116:119], v[84:87], v[28:31], v[116:119]\n"
    "ds_read_b64_tr_b16 v[22:23], v12 offset:41984\n"
    "v_cvt_pk_bf16_f32 v201, v106, v107\n"
    "v_mfma_f32_16x16x32_bf16 v[120:123], v[88:91], v[28:31], v[120:123]\n"
    "v_cvt_pk_bf16_f32 v202, v108, v109\n"
    "v_mfma_f32_16x16x32_bf16 v[124:127], v[92:95], v[28:31], v[124:127]\n"
    "v_cvt_pk_bf16_f32 v203, v110, v111\n"
    ".Lpad_2c_1:\n"
    "s_cbranch_execz .Lpad_2c_2\n"
    "s_waitcnt lgkmcnt(2)\n"
    "v_mfma_f32_16x16x32_bf16 v[96:99], v[32:35], v[14:17], 0\n"
    "ds_read_b64_tr_b16 v[24:25], v12 offset:49152\n"
    "v_mfma_f32_16x16x32_bf16 v[100:103], v[36:39], v[14:17], 0\n"
    "ds_read_b64_tr_b16 v[26:27], v12 offset:50176\n"
    "v_mfma_f32_16x16x32_bf16 v[104:107], v[40:43], v[14:17], 0\n"
    "v_mfma_f32_16x16x32_bf16 v[108:111], v[44:47], v[14:17], 0\n"
    "s_waitcnt lgkmcnt(2)\n"
    "v_mfma_f32_16x16x32_bf16 v[96:99], v[48:51], v[20:23], v[96:99]\n"
    "ds_read_b64_tr_b16 v[28:29], v12 offset:57344\n"
    "v_mfma_f32_16x16x32_bf16 v[100:103], v[52:55], v[20:23], v[100:103]\n"
    "ds_read_b64_tr_b16 v[30:31], v12 offset:58368\n"
    "v_mfma_f32_16x16x32_bf16 v[104:107], v[56:59], v[20:23], v[104:107]\n"
    "v_cvt_pk_bf16_f32 v140, v112, v113\n"
    "v_mfma_f32_16x16x32_bf16 v[108:111], v[60:63], v[20:23], v[108:111]\n"
    "v_cvt_pk_bf16_f32 v141, v114, v115\n"
    "s_waitcnt lgkmcnt(2)\n"
    "v_mfma_f32_16x16x32_bf16 v[96:99], v[64:67], v[24:27], v[96:99]\n"
    "ds_read_b64_tr_b16 v[14:15], v13 offset:32768\n"
    "v_mfma_f32_16x16x32_bf16 v[100:103], v[68:71], v[24:27], v[100:103]\n"
    "ds_read_b64_tr_b16 v[16:17], v13 offset:33792\n"
    "v_mfma_f32_16x16x32_bf16 v[104:107], v[72:75], v[24:27], v[104:107]\n"
    "v_cvt_pk_bf16_f32 v142, v116, v117\n"
    "v_mfma_f32_16x16x32_bf16 v[108:111], v[76:79], v[24:27], v[108:111]\n"
    "v_cvt_pk_bf16_f32 v143, v118, v119\n"
    "s_waitcnt lgkmcnt(2)\n"
    "v_mfma_f32_16x16x32_bf16 v[96:99], v[80:83], v[28:31], v[96:99]\n"
    "ds_read_b64_tr_b16 v[20:21], v13 offset:40960\n"
    "v_cvt_pk_bf16_f32 v204, v120, v121\n"
    "v_mfma_f32_16x16x32_bf16 v[100:103], v[84:87], v[28:31], v[100:103]\n"
    "ds_read_b64_tr_b16 v[22:23], v13 offset:41984\n"
    "v_cvt_pk_bf16_f32 v205, v122, v123\n"
    "v_mfma_f32_16x16x32_bf16 v[104:107], v[88:91], v[28:31], v[104:107]\n"
    "v_cvt_pk_bf16_f32 v206, v124, v125\n"
    "v_mfma_f32_16x16x32_bf16 v[108:111], v[92:95], v[28:31], v[108:111]\n"
    "v_cvt_pk_bf16_f32 v207, v126, v127\n"
    "s_waitcnt lgkmcnt(2)\n"
    "v_mfma_f32_16x16x32_bf16 v[112:115], v[32:35], v[14:17], 0\n"
    "ds_read_b64_tr_b16 v[24:25], v13 offset:49152\n"
    "v_mfma_f32_16x16x32_bf16 v[116:119], v[36:39], v[14:17], 0\n"
    "ds_read_b64_tr_b16 v[26:27], v13 offset:50176\n"
    "v_mfma_f32_16x16x32_bf16 v[120:123], v[40:43], v[14:17], 0\n"
    "v_mfma_f32_16x16x32_bf16 v[124:127], v[44:47], v[14:17], 0\n"
    "s_barrier\n"
    "s_waitcnt lgkmcnt(2)\n"
    "v_mfma_f32_16x16x32_bf16 v[112:115], v[48:51], v[20:23], v[112:115]\n"
    "ds_read_b64_tr_b16 v[28:29], v13 offset:57344\n"
    "ds_read_b128 v[32:35], v4 offset:32768\n"
    "v_mfma_f32_16x16x32_bf16 v[116:119], v[52:55], v[20:23], v[116:119]\n"
    "ds_read_b64_tr_b16 v[30:31], v13 offset:58368\n"
    "ds_read_b128 v[36:39], v4 offset:36864\n"
    "v_mfma_f32_16x16x32_bf16 v[120:123], v[56:59], v[20:23], v[120:123]\n"
    "ds_read_b128 v[40:43], v4 offset:40960\n"
    "v_cvt_pk_bf16_f32 v168, v96, v97\n"
    "v_mfma_f32_16x16x32_bf16 v[124:127], v[60:63], v[20:23], v[124:127]\n"
    "ds_read_b128 v[44:47], v4 offset:45056\n"
    "v_cvt_pk_bf16_f32 v169, v98, v99\n"
    "s_waitcnt lgkmcnt(6)\n"
    "v_mfma_f32_16x16x32_bf16 v[112:115], v[64:67], v[24:27], v[112:115]\n"
    "v_xor_b32 v10, 0x10000, v10\n"
    "ds_read_b64_tr_b16 v[14:15], v10 offset:0\n"
    "ds_read_b128 v[48:51], v5 offset:32768\n"
    "v_mfma_f32_16x16x32_bf16 v[116:119], v[68:71], v[24:27], v[116:119]\n"
    "ds_read_b64_tr_b16 v[16:17], v10 offset:1024\n"
    "ds_read_b128 v[52:55], v5 offset:36864\n"
    "v_mfma_f32_16x16x32_bf16 v[120:123], v[72:75], v[24:27], v[120:123]\n"
    "ds_read_b128 v[56:59], v5 offset:40960\n"
    "v_cvt_pk_bf16_f32 v170, v100, v101\n"
    "v_mfma_f32_16x16x32_bf16 v[124:127], v[76:79], v[24:27], v[124:127]\n"
    "ds_read_b128 v[60:63], v5 offset:45056\n"
    "v_cvt_pk_bf16_f32 v171, v102, v103\n"
    "s_waitcnt lgkmcnt(9)\n"
    "v_mfma_f32_16x16x32_bf16 v[112:115], v[80:83], v[28:31], v[112:115]\n"
    "ds_read_b64_tr_b16 v[20:21], v10 offset:8192\n"
    "ds_read_b128 v[64:67], v6 offset:32768\n"
    "v_cvt_pk_bf16_f32 v232, v104, v105\n"
    "v_mfma_f32_16x16x32_bf16 v[116:119], v[84:87], v[28:31], v[116:119]\n"
    "ds_read_b64_tr_b16 v[22:23], v10 offset:9216\n"
    "ds_read_b128 v[68:71], v6 offset:36864\n"
    "v_cvt_pk_bf16_f32 v233, v106, v107\n"
    "v_mfma_f32_16x16x32_bf16 v[120:123], v[88:91], v[28:31], v[120:123]\n"
    "ds_read_b128 v[72:75], v6 offset:40960\n"
    "v_cvt_pk_bf16_f32 v234, v108, v109\n"
    "v_mfma_f32_16x16x32_bf16 v[124:127], v[92:95], v[28:31], v[124:127]\n"
    "ds_read_b128 v[76:79], v6 offset:45056\n"
    "v_cvt_pk_bf16_f32 v235, v110, v111\n"
    "s_waitcnt lgkmcnt(9)\n"
    "v_mfma_f32_16x16x32_bf16 v[96:99], v[32:35], v[14:17], 0\n"
    "ds_read_b64_tr_b16 v[24:25], v10 offset:16384\n"
    "ds_read_b128 v[80:83], v7 offset:32768\n"
    "v_mfma_f32_16x16x32_bf16 v[100:103], v[36:39], v[14:17], 0\n"
    "ds_read_b64_tr_b16 v[26:27], v10 offset:17408\n"
    "ds_read_b128 v[84:87], v7 offset:36864\n"
    "v_mfma_f32_16x16x32_bf16 v[104:107], v[40:43], v[14:17], 0\n"
    "ds_read_b128 v[88:91], v7 offset:40960\n"
    "v_mfma_f32_16x16x32_bf16 v[108:111], v[44:47], v[14:17], 0\n"
    "ds_read_b128 v[92:95], v7 offset:45056\n"
    "s_waitcnt lgkmcnt(9)\n"
    "v_mfma_f32_16x16x32_bf16 v[96:99], v[48:51], v[20:23], v[96:99]\n"
    "ds_read_b64_tr_b16 v[28:29], v10 offset:24576\n"
    "v_mfma_f32_16x16x32_bf16 v[100:103], v[52:55], v[20:23], v[100:103]\n"
    "ds_read_b64_tr_b16 v[30:31], v10 offset:25600\n"
    "v_mfma_f32_16x16x32_bf16 v[104:107], v[56:59], v[20:23], v[104:107]\n"
    "v_cvt_pk_bf16_f32 v172, v112, v113\n"
    "v_mfma_f32_16x16x32_bf16 v[108:111], v[60:63], v[20:23], v[108:111]\n"
    "v_cvt_pk_bf16_f32 v173, v114, v115\n"
    "s_waitcnt lgkmcnt(5)\n"
    "v_mfma_f32_16x16x32_bf16 v[96:99], v[64:67], v[24:27], v[96:99]\n"
    "v_xor_b32 v11, 0x10000, v11\n"
    "ds_read_b64_tr_b16 v[14:15], v11 offset:0\n"
    "v_mfma_f32_16x16x32_bf16 v[100:103], v[68:71], v[24:27], v[100:103]\n"
    "ds_read_b64_tr_b16 v[16:17], v11 offset:1024\n"
    "v_mfma_f32_16x16x32_bf16 v[104:107], v[72:75], v[24:27], v[104:107]\n"
    "v_cvt_pk_bf16_f32 v174, v116, v117\n"
    "v_mfma_f32_16x16x32_bf16 v[108:111], v[76:79], v[24:27], v[108:111]\n"
    "v_cvt_pk_bf16_f32 v175, v118, v119\n"
    "s_waitcnt lgkmcnt(2)\n"
    "v_mfma_f32_16x16x32_bf16 v[96:99], v[80:83], v[28:31], v[96:99]\n"
    "ds_read_b64_tr_b16 v[20:21], v11 offset:8192\n"
    "v_cvt_pk_bf16_f32 v236, v120, v121\n"
    "v_mfma_f32_16x16x32_bf16 v[100:103], v[84:87], v[28:31], v[100:103]\n"
    "ds_read_b64_tr_b16 v[22:23], v11 offset:9216\n"
    "v_cvt_pk_bf16_f32 v237, v122, v123\n"
    "v_mfma_f32_16x16x32_bf16 v[104:107], v[88:91], v[28:31], v[104:107]\n"
    "v_cvt_pk_bf16_f32 v238, v124, v125\n"
    "v_mfma_f32_16x16x32_bf16 v[108:111], v[92:95], v[28:31], v[108:111]\n"
    "v_cvt_pk_bf16_f32 v239, v126, v127\n"
    ".Lpad_2d_0:\n"
    "s_cbranch_execz .Lpad_2d_1\n"
    "s_waitcnt lgkmcnt(2)\n"
    "v_mfma_f32_16x16x32_bf16 v[112:115], v[32:35], v[14:17], 0\n"
    "ds_read_b64_tr_b16 v[24:25], v11 offset:16384\n"
    "v_mfma_f32_16x16x32_bf16 v[116:119], v[36:39], v[14:17], 0\n"
    "ds_read_b64_tr_b16 v[26:27], v11 offset:17408\n"
    "v_mfma_f32_16x16x32_bf16 v[120:123], v[40:43], v[14:17], 0\n"
    "v_mfma_f32_16x16x32_bf16 v[124:127], v[44:47], v[14:17], 0\n"
    "s_waitcnt lgkmcnt(2)\n"
    "v_mfma_f32_16x16x32_bf16 v[112:115], v[48:51], v[20:23], v[112:115]\n"
    "ds_read_b64_tr_b16 v[28:29], v11 offset:24576\n"
    "v_mfma_f32_16x16x32_bf16 v[116:119], v[52:55], v[20:23], v[116:119]\n"
    "ds_read_b64_tr_b16 v[30:31], v11 offset:25600\n"
    "v_mfma_f32_16x16x32_bf16 v[120:123], v[56:59], v[20:23], v[120:123]\n"
    "v_cvt_pk_bf16_f32 v144, v96, v97\n"
    "v_mfma_f32_16x16x32_bf16 v[124:127], v[60:63], v[20:23], v[124:127]\n"
    "v_cvt_pk_bf16_f32 v145, v98, v99\n"
    "s_waitcnt lgkmcnt(2)\n"
    "v_mfma_f32_16x16x32_bf16 v[112:115], v[64:67], v[24:27], v[112:115]\n"
    "s_barrier\n"
    "v_xor_b32 v12, 0x10000, v12\n"
    "ds_read_b64_tr_b16 v[14:15], v12 offset:0\n"
    "v_mfma_f32_16x16x32_bf16 v[116:119], v[68:71], v[24:27], v[116:119]\n"
    "ds_read_b64_tr_b16 v[16:17], v12 offset:1024\n"
    "v_mfma_f32_16x16x32_bf16 v[120:123], v[72:75], v[24:27], v[120:123]\n"
    "v_cvt_pk_bf16_f32 v146, v100, v101\n"
    "v_mfma_f32_16x16x32_bf16 v[124:127], v[76:79], v[24:27], v[124:127]\n"
    "v_cvt_pk_bf16_f32 v147, v102, v103\n"
    "s_waitcnt lgkmcnt(2)\n"
    "v_mfma_f32_16x16x32_bf16 v[112:115], v[80:83], v[28:31], v[112:115]\n"
    "ds_read_b64_tr_b16 v[20:21], v12 offset:8192\n"
    "v_cvt_pk_bf16_f32 v208, v104, v105\n"
    "v_mfma_f32_16x16x32_bf16 v[116:119], v[84:87], v[28:31], v[116:119]\n"
    "ds_read_b64_tr_b16 v[22:23], v12 offset:9216\n"
    "v_cvt_pk_bf16_f32 v209, v106, v107\n"
    "v_mfma_f32_16x16x32_bf16 v[120:123], v[88:91], v[28:31], v[120:123]\n"
    "v_cvt_pk_bf16_f32 v210, v108, v109\n"
    "v_mfma_f32_16x16x32_bf16 v[124:127], v[92:95], v[28:31], v[124:127]\n"
    "v_cvt_pk_bf16_f32 v211, v110, v111\n"
    "s_waitcnt lgkmcnt(2)\n"
    "v_mfma_f32_16x16x32_bf16 v[96:99], v[32:35], v[14:17], 0\n"
    "ds_read_b64_tr_b16 v[24:25], v12 offset:16384\n"
    "v_mfma_f32_16x16x32_bf16 v[100:103], v[36:39], v[14:17], 0\n"
    "ds_read_b64_tr_b16 v[26:27], v12 offset:17408\n"
    "v_mfma_f32_16x16x32_bf16 v[104:107], v[40:43], v[14:17], 0\n"
    "v_mfma_f32_16x16x32_bf16 v[108:111], v[44:47], v[14:17], 0\n"
    "s_waitcnt lgkmcnt(2)\n"
    "v_mfma_f32_16x16x32_bf16 v[96:99], v[48:51], v[20:23], v[96:99]\n"
    "ds_read_b64_tr_b16 v[28:29], v12 offset:24576\n"
    "v_mfma_f32_16x16x32_bf16 v[100:103], v[52:55], v[20:23], v[100:103]\n"
    "ds_read_b64_tr_b16 v[30:31], v12 offset:25600\n"
    "v_mfma_f32_16x16x32_bf16 v[104:107], v[56:59], v[20:23], v[104:107]\n"
    "v_cvt_pk_bf16_f32 v148, v112, v113\n"
    "v_mfma_f32_16x16x32_bf16 v[108:111], v[60:63], v[20:23], v[108:111]\n"
    "v_cvt_pk_bf16_f32 v149, v114, v115\n"
    "s_waitcnt lgkmcnt(2)\n"
    "v_mfma_f32_16x16x32_bf16 v[96:99], v[64:67], v[24:27], v[96:99]\n"
    "v_xor_b32 v13, 0x10000, v13\n"
    "ds_read_b64_tr_b16 v[14:15], v13 offset:0\n"
    "v_mfma_f32_16x16x32_bf16 v[100:103], v[68:71], v[24:27], v[100:103]\n"
    "ds_read_b64_tr_b16 v[16:17], v13 offset:1024\n"
    "v_mfma_f32_16x16x32_bf16 v[104:107], v[72:75], v[24:27], v[104:107]\n"
    "v_cvt_pk_bf16_f32 v150, v116, v117\n"
    "v_mfma_f32_16x16x32_bf16 v[108:111], v[76:79], v[24:27], v[108:111]\n"
    "v_cvt_pk_bf16_f32 v151, v118, v119\n"
    "s_waitcnt lgkmcnt(2)\n"
    "v_mfma_f32_16x16x32_bf16 v[96:99], v[80:83], v[28:31], v[96:99]\n"
    "ds_read_b64_tr_b16 v[20:21], v13 offset:8192\n"
    "v_cvt_pk_bf16_f32 v212, v120, v121\n"
    "v_mfma_f32_16x16x32_bf16 v[100:103], v[84:87], v[28:31], v[100:103]\n"
    "ds_read_b64_tr_b16 v[22:23], v13 offset:9216\n"
    "v_cvt_pk_bf16_f32 v213, v122, v123\n"
    "v_mfma_f32_16x16x32_bf16 v[104:107], v[88:91], v[28:31], v[104:107]\n"
    "v_cvt_pk_bf16_f32 v214, v124, v125\n"
    "v_mfma_f32_16x16x32_bf16 v[108:111], v[92:95], v[28:31], v[108:111]\n"
    "v_cvt_pk_bf16_f32 v215, v126, v127\n"
    "s_waitcnt lgkmcnt(2)\n"
    "v_mfma_f32_16x16x32_bf16 v[112:115], v[32:35], v[14:17], 0\n"
    "ds_read_b64_tr_b16 v[24:25], v13 offset:16384\n"
    "v_mfma_f32_16x16x32_bf16 v[116:119], v[36:39], v[14:17], 0\n"
    "ds_read_b64_tr_b16 v[26:27], v13 offset:17408\n"
    "v_mfma_f32_16x16x32_bf16 v[120:123], v[40:43], v[14:17], 0\n"
    "v_mfma_f32_16x16x32_bf16 v[124:127], v[44:47], v[14:17], 0\n"
    "s_waitcnt lgkmcnt(2)\n"
    "v_mfma_f32_16x16x32_bf16 v[112:115], v[48:51], v[20:23], v[112:115]\n"
    "ds_read_b64_tr_b16 v[28:29], v13 offset:24576\n"
    "v_mfma_f32_16x16x32_bf16 v[116:119], v[52:55], v[20:23], v[116:119]\n"
    "ds_read_b64_tr_b16 v[30:31], v13 offset:25600\n"
    "v_mfma_f32_16x16x32_bf16 v[120:123], v[56:59], v[20:23], v[120:123]\n"
    "v_cvt_pk_bf16_f32 v176, v96, v97\n"
    "v_mfma_f32_16x16x32_bf16 v[124:127], v[60:63], v[20:23], v[124:127]\n"
    "v_cvt_pk_bf16_f32 v177, v98, v99\n"
    "s_waitcnt lgkmcnt(2)\n"
    "v_mfma_f32_16x16x32_bf16 v[112:115], v[64:67], v[24:27], v[112:115]\n"
    "ds_read_b64_tr_b16 v[14:15], v10 offset:32768\n"
    "v_mfma_f32_16x16x32_bf16 v[116:119], v[68:71], v[24:27], v[116:119]\n"
    "ds_read_b64_tr_b16 v[16:17], v10 offset:33792\n"
    "v_mfma_f32_16x16x32_bf16 v[120:123], v[72:75], v[24:27], v[120:123]\n"
    "v_cvt_pk_bf16_f32 v178, v100, v101\n"
    "v_mfma_f32_16x16x32_bf16 v[124:127], v[76:79], v[24:27], v[124:127]\n"
    "v_cvt_pk_bf16_f32 v179, v102, v103\n"
    "s_waitcnt lgkmcnt(2)\n"
    "v_mfma_f32_16x16x32_bf16 v[112:115], v[80:83], v[28:31], v[112:115]\n"
    "ds_read_b64_tr_b16 v[20:21], v10 offset:40960\n"
    "v_cvt_pk_bf16_f32 v240, v104, v105\n"
    "v_mfma_f32_16x16x32_bf16 v[116:119], v[84:87], v[28:31], v[116:119]\n"
    "ds_read_b64_tr_b16 v[22:23], v10 offset:41984\n"
    "v_cvt_pk_bf16_f32 v241, v106, v107\n"
    "v_mfma_f32_16x16x32_bf16 v[120:123], v[88:91], v[28:31], v[120:123]\n"
    "v_cvt_pk_bf16_f32 v242, v108, v109\n"
    "v_mfma_f32_16x16x32_bf16 v[124:127], v[92:95], v[28:31], v[124:127]\n"
    "v_cvt_pk_bf16_f32 v243, v110, v111\n"
    ".Lpad_2a_1:\n"
    "s_cbranch_execz .Lpad_2a_2\n"
    "s_waitcnt lgkmcnt(2)\n"
    "v_mfma_f32_16x16x32_bf16 v[96:99], v[32:35], v[14:17], 0\n"
    "ds_read_b64_tr_b16 v[24:25], v10 offset:49152\n"
    "v_mfma_f32_16x16x32_bf16 v[100:103], v[36:39], v[14:17], 0\n"
    "ds_read_b64_tr_b16 v[26:27], v10 offset:50176\n"
    "v_mfma_f32_16x16x32_bf16 v[104:107], v[40:43], v[14:17], 0\n"
    "v_mfma_f32_16x16x32_bf16 v[108:111], v[44:47], v[14:17], 0\n"
    "s_waitcnt lgkmcnt(2)\n"
    "v_mfma_f32_16x16x32_bf16 v[96:99], v[48:51], v[20:23], v[96:99]\n"
    "ds_read_b64_tr_b16 v[28:29], v10 offset:57344\n"
    "v_mfma_f32_16x16x32_bf16 v[100:103], v[52:55], v[20:23], v[100:103]\n"
    "ds_read_b64_tr_b16 v[30:31], v10 offset:58368\n"
    "v_mfma_f32_16x16x32_bf16 v[104:107], v[56:59], v[20:23], v[104:107]\n"
    "v_cvt_pk_bf16_f32 v180, v112, v113\n"
    "v_mfma_f32_16x16x32_bf16 v[108:111], v[60:63], v[20:23], v[108:111]\n"
    "v_cvt_pk_bf16_f32 v181, v114, v115\n"
    "s_waitcnt lgkmcnt(2)\n"
    "v_mfma_f32_16x16x32_bf16 v[96:99], v[64:67], v[24:27], v[96:99]\n"
    "ds_read_b64_tr_b16 v[14:15], v11 offset:32768\n"
    "v_mfma_f32_16x16x32_bf16 v[100:103], v[68:71], v[24:27], v[100:103]\n"
    "ds_read_b64_tr_b16 v[16:17], v11 offset:33792\n"
    "v_mfma_f32_16x16x32_bf16 v[104:107], v[72:75], v[24:27], v[104:107]\n"
    "v_cvt_pk_bf16_f32 v182, v116, v117\n"
    "v_mfma_f32_16x16x32_bf16 v[108:111], v[76:79], v[24:27], v[108:111]\n"
    "v_cvt_pk_bf16_f32 v183, v118, v119\n"
    "s_waitcnt lgkmcnt(2)\n"
    "v_mfma_f32_16x16x32_bf16 v[96:99], v[80:83], v[28:31], v[96:99]\n"
    "ds_read_b64_tr_b16 v[20:21], v11 offset:40960\n"
    "v_cvt_pk_bf16_f32 v244, v120, v121\n"
    "v_mfma_f32_16x16x32_bf16 v[100:103], v[84:87], v[28:31], v[100:103]\n"
    "ds_read_b64_tr_b16 v[22:23], v11 offset:41984\n"
    "v_cvt_pk_bf16_f32 v245, v122, v123\n"
    "v_mfma_f32_16x16x32_bf16 v[104:107], v[88:91], v[28:31], v[104:107]\n"
    "v_cvt_pk_bf16_f32 v246, v124, v125\n"
    "v_mfma_f32_16x16x32_bf16 v[108:111], v[92:95], v[28:31], v[108:111]\n"
    "v_cvt_pk_bf16_f32 v247, v126, v127\n"
    "s_waitcnt lgkmcnt(2)\n"
    "v_mfma_f32_16x16x32_bf16 v[112:115], v[32:35], v[14:17], 0\n"
    "ds_read_b64_tr_b16 v[24:25], v11 offset:49152\n"
    "v_mfma_f32_16x16x32_bf16 v[116:119], v[36:39], v[14:17], 0\n"
    "ds_read_b64_tr_b16 v[26:27], v11 offset:50176\n"
    "v_mfma_f32_16x16x32_bf16 v[120:123], v[40:43], v[14:17], 0\n"
    "v_mfma_f32_16x16x32_bf16 v[124:127], v[44:47], v[14:17], 0\n"
    "s_waitcnt lgkmcnt(2)\n"
    "v_mfma_f32_16x16x32_bf16 v[112:115], v[48:51], v[20:23], v[112:115]\n"
    "ds_read_b64_tr_b16 v[28:29], v11 offset:57344\n"
    "v_mfma_f32_16x16x32_bf16 v[116:119], v[52:55], v[20:23], v[116:119]\n"
    "ds_read_b64_tr_b16 v[30:31], v11 offset:58368\n"
    "v_mfma_f32_16x16x32_bf16 v[120:123], v[56:59], v[20:23], v[120:123]\n"
    "v_cvt_pk_bf16_f32 v152, v96, v97\n"
    "v_mfma_f32_16x16x32_bf16 v[124:127], v[60:63], v[20:23], v[124:127]\n"
    "v_cvt_pk_bf16_f32 v153, v98, v99\n"
    "s_waitcnt lgkmcnt(2)\n"
    "v_mfma_f32_16x16x32_bf16 v[112:115], v[64:67], v[24:27], v[112:115]\n"
    "ds_read_b64_tr_b16 v[14:15], v12 offset:32768\n"
    "v_mfma_f32_16x16x32_bf16 v[116:119], v[68:71], v[24:27], v[116:119]\n"
    "ds_read_b64_tr_b16 v[16:17], v12 offset:33792\n"
    "v_mfma_f32_16x16x32_bf16 v[120:123], v[72:75], v[24:27], v[120:123]\n"
    "v_cvt_pk_bf16_f32 v154, v100, v101\n"
    "v_mfma_f32_16x16x32_bf16 v[124:127], v[76:79], v[24:27], v[124:127]\n"
    "v_cvt_pk_bf16_f32 v155, v102, v103\n"
    "s_waitcnt lgkmcnt(2)\n"
    "v_mfma_f32_16x16x32_bf16 v[112:115], v[80:83], v[28:31], v[112:115]\n"
    "ds_read_b64_tr_b16 v[20:21], v12 offset:40960\n"
    "v_cvt_pk_bf16_f32 v216, v104, v105\n"
    "v_mfma_f32_16x16x32_bf16 v[116:119], v[84:87], v[28:31], v[116:119]\n"
    "ds_read_b64_tr_b16 v[22:23], v12 offset:41984\n"
    "v_cvt_pk_bf16_f32 v217, v106, v107\n"
    "v_mfma_f32_16x16x32_bf16 v[120:123], v[88:91], v[28:31], v[120:123]\n"
    "v_cvt_pk_bf16_f32 v218, v108, v109\n"
    "v_mfma_f32_16x16x32_bf16 v[124:127], v[92:95], v[28:31], v[124:127]\n"
    "v_cvt_pk_bf16_f32 v219, v110, v111\n"
    "s_waitcnt lgkmcnt(2)\n"
    "v_mfma_f32_16x16x32_bf16 v[96:99], v[32:35], v[14:17], 0\n"
    "ds_read_b64_tr_b16 v[24:25], v12 offset:49152\n"
    "v_mfma_f32_16x16x32_bf16 v[100:103], v[36:39], v[14:17], 0\n"
    "ds_read_b64_tr_b16 v[26:27], v12 offset:50176\n"
    "v_mfma_f32_16x16x32_bf16 v[104:107], v[40:43], v[14:17], 0\n"
    "v_mfma_f32_16x16x32_bf16 v[108:111], v[44:47], v[14:17], 0\n"
    "s_waitcnt lgkmcnt(2)\n"
    "v_mfma_f32_16x16x32_bf16 v[96:99], v[48:51], v[20:23], v[96:99]\n"
    "ds_read_b64_tr_b16 v[28:29], v12 offset:57344\n"
    "v_mfma_f32_16x16x32_bf16 v[100:103], v[52:55], v[20:23], v[100:103]\n"
    "ds_read_b64_tr_b16 v[30:31], v12 offset:58368\n"
    "v_mfma_f32_16x16x32_bf16 v[104:107], v[56:59], v[20:23], v[104:107]\n"
    "v_cvt_pk_bf16_f32 v156, v112, v113\n"
    "v_mfma_f32_16x16x32_bf16 v[108:111], v[60:63], v[20:23], v[108:111]\n"
    "v_cvt_pk_bf16_f32 v157, v114, v115\n"
    "s_waitcnt lgkmcnt(2)\n"
    "v_mfma_f32_16x16x32_bf16 v[96:99], v[64:67], v[24:27], v[96:99]\n"
    "ds_read_b64_tr_b16 v[14:15], v13 offset:32768\n"
    "v_mfma_f32_16x16x32_bf16 v[100:103], v[68:71], v[24:27], v[100:103]\n"
    "ds_read_b64_tr_b16 v[16:17], v13 offset:33792\n"
    "v_mfma_f32_16x16x32_bf16 v[104:107], v[72:75], v[24:27], v[104:107]\n"
    "v_cvt_pk_bf16_f32 v158, v116, v117\n"
    "v_mfma_f32_16x16x32_bf16 v[108:111], v[76:79], v[24:27], v[108:111]\n"
    "v_cvt_pk_bf16_f32 v159, v118, v119\n"
    "s_waitcnt lgkmcnt(2)\n"
    "v_mfma_f32_16x16x32_bf16 v[96:99], v[80:83], v[28:31], v[96:99]\n"
    "ds_read_b64_tr_b16 v[20:21], v13 offset:40960\n"
    "v_cvt_pk_bf16_f32 v220, v120, v121\n"
    "v_mfma_f32_16x16x32_bf16 v[100:103], v[84:87], v[28:31], v[100:103]\n"
    "ds_read_b64_tr_b16 v[22:23], v13 offset:41984\n"
    "v_cvt_pk_bf16_f32 v221, v122, v123\n"
    "v_mfma_f32_16x16x32_bf16 v[104:107], v[88:91], v[28:31], v[104:107]\n"
    "v_cvt_pk_bf16_f32 v222, v124, v125\n"
    "v_mfma_f32_16x16x32_bf16 v[108:111], v[92:95], v[28:31], v[108:111]\n"
    "v_cvt_pk_bf16_f32 v223, v126, v127\n"
    ".Lpad_2b_2:\n"
    "s_cbranch_execz .Lpad_2b_3\n"
    "s_waitcnt lgkmcnt(2)\n"
    "v_mfma_f32_16x16x32_bf16 v[112:115], v[32:35], v[14:17], 0\n"
    "ds_read_b64_tr_b16 v[24:25], v13 offset:49152\n"
    "v_mfma_f32_16x16x32_bf16 v[116:119], v[36:39], v[14:17], 0\n"
    "ds_read_b64_tr_b16 v[26:27], v13 offset:50176\n"
    "v_mfma_f32_16x16x32_bf16 v[120:123], v[40:43], v[14:17], 0\n"
    "v_mfma_f32_16x16x32_bf16 v[124:127], v[44:47], v[14:17], 0\n"
    "s_waitcnt lgkmcnt(2)\n"
    "v_mfma_f32_16x16x32_bf16 v[112:115], v[48:51], v[20:23], v[112:115]\n"
    "ds_read_b64_tr_b16 v[28:29], v13 offset:57344\n"
    "v_mfma_f32_16x16x32_bf16 v[116:119], v[52:55], v[20:23], v[116:119]\n"
    "ds_read_b64_tr_b16 v[30:31], v13 offset:58368\n"
    "v_mfma_f32_16x16x32_bf16 v[120:123], v[56:59], v[20:23], v[120:123]\n"
    "v_cvt_pk_bf16_f32 v184, v96, v97\n"
    "v_mfma_f32_16x16x32_bf16 v[124:127], v[60:63], v[20:23], v[124:127]\n"
    "v_cvt_pk_bf16_f32 v185, v98, v99\n"
    "s_barrier\n"
    "s_waitcnt lgkmcnt(2)\n"
    "v_mfma_f32_16x16x32_bf16 v[112:115], v[64:67], v[24:27], v[112:115]\n"
    "ds_read_b128 v[32:35], v19 offset:0\n"
    "v_mfma_f32_16x16x32_bf16 v[116:119], v[68:71], v[24:27], v[116:119]\n"
    "ds_read_b128 v[36:39], v19 offset:1024\n"
    "v_mfma_f32_16x16x32_bf16 v[120:123], v[72:75], v[24:27], v[120:123]\n"
    "ds_read_b128 v[40:43], v19 offset:2048\n"
    "v_cvt_pk_bf16_f32 v186, v100, v101\n"
    "v_mfma_f32_16x16x32_bf16 v[124:127], v[76:79], v[24:27], v[124:127]\n"
    "ds_read_b128 v[44:47], v19 offset:3072\n"
    "v_cvt_pk_bf16_f32 v187, v102, v103\n"
    "s_waitcnt lgkmcnt(4)\n"
    "v_mfma_f32_16x16x32_bf16 v[112:115], v[80:83], v[28:31], v[112:115]\n"
    "ds_read_b128 v[48:51], v19 offset:4096\n"
    "v_cvt_pk_bf16_f32 v248, v104, v105\n"
    "v_mfma_f32_16x16x32_bf16 v[116:119], v[84:87], v[28:31], v[116:119]\n"
    "ds_read_b128 v[52:55], v19 offset:5120\n"
    "v_cvt_pk_bf16_f32 v249, v106, v107\n"
    "v_mfma_f32_16x16x32_bf16 v[120:123], v[88:91], v[28:31], v[120:123]\n"
    "ds_read_b128 v[56:59], v19 offset:6144\n"
    "v_cvt_pk_bf16_f32 v250, v108, v109\n"
    "v_mfma_f32_16x16x32_bf16 v[124:127], v[92:95], v[28:31], v[124:127]\n"
    "ds_read_b128 v[60:63], v19 offset:7168\n"
    "v_cvt_pk_bf16_f32 v251, v110, v111\n"
    "s_waitcnt lgkmcnt(6)\n"
    "v_mfma_f32_16x16x32_bf16 v[96:99], v[128:131], v[32:35], 0\n"
    ".Lpad_2a_2:\n"
    "s_cbranch_execz .Lpad_2a_3\n"
    "v_mov_b64 v[4:5], s[96:97]\n"
    "v_mfma_f32_16x16x32_bf16 v[100:103], v[132:135], v[32:35], 0\n"
    "v_mov_b64 v[6:7], s[98:99]\n"
    "s_waitcnt lgkmcnt(4)\n"
    "v_mfma_f32_16x16x32_bf16 v[96:99], v[136:139], v[40:43], v[96:99]\n"
    "v_mov_b64 v[8:9], s[58:59]\n"
    "v_mfma_f32_16x16x32_bf16 v[100:103], v[140:143], v[40:43], v[100:103]\n"
    "v_mov_b64 v[10:11], s[4:5]\n"
    "s_waitcnt lgkmcnt(2)\n"
    "v_mfma_f32_16x16x32_bf16 v[96:99], v[144:147], v[48:51], v[96:99]\n"
    "v_cvt_pk_bf16_f32 v188, v112, v113\n"
    "v_cvt_pk_bf16_f32 v189, v114, v115\n"
    "v_mfma_f32_16x16x32_bf16 v[100:103], v[148:151], v[48:51], v[100:103]\n"
    "v_cvt_pk_bf16_f32 v190, v116, v117\n"
    "v_cvt_pk_bf16_f32 v191, v118, v119\n"
    "s_waitcnt lgkmcnt(0)\n"
    "v_mfma_f32_16x16x32_bf16 v[96:99], v[152:155], v[56:59], v[96:99]\n"
    "v_cvt_pk_bf16_f32 v252, v120, v121\n"
    "v_cvt_pk_bf16_f32 v253, v122, v123\n"
    "v_mfma_f32_16x16x32_bf16 v[100:103], v[156:159], v[56:59], v[100:103]\n"
    "v_cvt_pk_bf16_f32 v254, v124, v125\n"
    "v_cvt_pk_bf16_f32 v255, v126, v127\n"
    "v_mfma_f32_16x16x32_bf16 v[104:107], v[128:131], v[36:39], 0\n"
    ".Lpad_2b_3:\n"
    "s_cbranch_execz .Lpad_2b_4\n"
    "v_mfma_f32_16x16x32_bf16 v[108:111], v[132:135], v[36:39], 0\n"
    "s_nop 0\n"
    "v_mfma_f32_16x16x32_bf16 v[104:107], v[136:139], v[44:47], v[104:107]\n"
    "s_nop 0\n"
    "v_mfma_f32_16x16x32_bf16 v[108:111], v[140:143], v[44:47], v[108:111]\n"
    "v_cvt_pk_bf16_f32 v20, v96, v97\n"
    "v_mfma_f32_16x16x32_bf16 v[104:107], v[144:147], v[52:55], v[104:107]\n"
    "v_cvt_pk_bf16_f32 v21, v98, v99\n"
    "v_mfma_f32_16x16x32_bf16 v[108:111], v[148:151], v[52:55], v[108:111]\n"
    "v_cvt_pk_bf16_f32 v22, v100, v101\n"
    "v_mfma_f32_16x16x32_bf16 v[104:107], v[152:155], v[60:63], v[104:107]\n"
    "v_cvt_pk_bf16_f32 v23, v102, v103\n"
    "v_mfma_f32_16x16x32_bf16 v[108:111], v[156:159], v[60:63], v[108:111]\n"
    "buffer_store_dwordx4 v[20:23], v2, s[28:31], s54 offen offset:0 sc1\n"
    "v_mfma_f32_16x16x32_bf16 v[112:115], v[160:163], v[32:35], 0\n"
    ".Lpad_2c_2:\n"
    "s_cbranch_execz .Lpad_2c_3\n"
    "v_mfma_f32_16x16x32_bf16 v[116:119], v[164:167], v[32:35], 0\n"
    "s_nop 0\n"
    "v_mfma_f32_16x16x32_bf16 v[112:115], v[168:171], v[40:43], v[112:115]\n"
    "s_nop 0\n"
    "v_mfma_f32_16x16x32_bf16 v[116:119], v[172:175], v[40:43], v[116:119]\n"
    "v_cvt_pk_bf16_f32 v24, v104, v105\n"
    "v_mfma_f32_16x16x32_bf16 v[112:115], v[176:179], v[48:51], v[112:115]\n"
    "v_cvt_pk_bf16_f32 v25, v106, v107\n"
    "v_mfma_f32_16x16x32_bf16 v[116:119], v[180:183], v[48:51], v[116:119]\n"
    "v_cvt_pk_bf16_f32 v26, v108, v109\n"
    "v_mfma_f32_16x16x32_bf16 v[112:115], v[184:187], v[56:59], v[112:115]\n"
    "v_cvt_pk_bf16_f32 v27, v110, v111\n"
    "v_mfma_f32_16x16x32_bf16 v[116:119], v[188:191], v[56:59], v[116:119]\n"
    "buffer_store_dwordx4 v[24:27], v2, s[28:31], s54 offen offset:1024 sc1\n"
    "s_barrier\n"
    "v_mfma_f32_16x16x32_bf16 v[120:123], v[160:163], v[36:39], 0\n"
    ".Lpad_2d_1:\n"
    "s_cbranch_execz .Lpad_2d_2\n"
    "ds_read_b128 v[64:67], v19 offset:8192\n"
    "v_mfma_f32_16x16x32_bf16 v[124:127], v[164:167], v[36:39], 0\n"
    "ds_read_b128 v[72:75], v19 offset:10240\n"
    "v_mfma_f32_16x16x32_bf16 v[120:123], v[168:171], v[44:47], v[120:123]\n"
    "ds_read_b128 v[80:83], v19 offset:12288\n"
    "v_mfma_f32_16x16x32_bf16 v[124:127], v[172:175], v[44:47], v[124:127]\n"
    "v_cvt_pk_bf16_f32 v128, v112, v113\n"
    "v_mfma_f32_16x16x32_bf16 v[120:123], v[176:179], v[52:55], v[120:123]\n"
    "v_cvt_pk_bf16_f32 v129, v114, v115\n"
    "v_mfma_f32_16x16x32_bf16 v[124:127], v[180:183], v[52:55], v[124:127]\n"
    "v_cvt_pk_bf16_f32 v130, v116, v117\n"
    "v_mfma_f32_16x16x32_bf16 v[120:123], v[184:187], v[60:63], v[120:123]\n"
    "v_cvt_pk_bf16_f32 v131, v118, v119\n"
    "v_mfma_f32_16x16x32_bf16 v[124:127], v[188:191], v[60:63], v[124:127]\n"
    "buffer_store_dwordx4 v[128:131], v2, s[28:31], s55 offen offset:0 sc1\n"
    "s_waitcnt lgkmcnt(2)\n"
    "v_mfma_f32_16x16x32_bf16 v[96:99], v[192:195], v[64:67], 0\n"
    ".Lpad_2a_3:\n"
    "s_cbranch_execz .Lpadret_2a\n"
    "ds_read_b128 v[88:91], v19 offset:14336\n"
    "v_mfma_f32_16x16x32_bf16 v[100:103], v[196:199], v[64:67], 0\n"
    "ds_read_b128 v[68:71], v19 offset:9216\n"
    "s_waitcnt lgkmcnt(3)\n"
    "v_mfma_f32_16x16x32_bf16 v[96:99], v[200:203], v[72:75], v[96:99]\n"
    "ds_read_b128 v[76:79], v19 offset:11264\n"
    "v_mfma_f32_16x16x32_bf16 v[100:103], v[204:207], v[72:75], v[100:103]\n"
    "v_cvt_pk_bf16_f32 v132, v120, v121\n"
    "s_waitcnt lgkmcnt(3)\n"
    "v_mfma_f32_16x16x32_bf16 v[96:99], v[208:211], v[80:83], v[96:99]\n"
    "v_cvt_pk_bf16_f32 v133, v122, v123\n"
    "v_mfma_f32_16x16x32_bf16 v[100:103], v[212:215], v[80:83], v[100:103]\n"
    "v_cvt_pk_bf16_f32 v134, v124, v125\n"
    "s_waitcnt lgkmcnt(2)\n"
    "v_mfma_f32_16x16x32_bf16 v[96:99], v[216:219], v[88:91], v[96:99]\n"
    "v_cvt_pk_bf16_f32 v135, v126, v127\n"
    "v_mfma_f32_16x16x32_bf16 v[100:103], v[220:223], v[88:91], v[100:103]\n"
    "buffer_store_dwordx4 v[132:135], v2, s[28:31], s55 offen offset:1024 sc1\n"
    "s_waitcnt lgkmcnt(1)\n"
    "v_mfma_f32_16x16x32_bf16 v[104:107], v[192:195], v[68:71], 0\n"
    ".Lpad_2b_4:\n"
    "s_cbranch_execz .Lpad_2b_5\n"
    "ds_read_b128 v[84:87], v19 offset:13312\n"
    "v_mfma_f32_16x16x32_bf16 v[108:111], v[196:199], v[68:71], 0\n"
    "ds_read_b128 v[92:95], v19 offset:15360\n"
    "s_waitcnt lgkmcnt(2)\n"
    "v_mfma_f32_16x16x32_bf16 v[104:107], v[200:203], v[76:79], v[104:107]\n"
    "s_nop 0\n"
    "v_mfma_f32_16x16x32_bf16 v[108:111], v[204:207], v[76:79], v[108:111]\n"
    "v_cvt_pk_bf16_f32 v160, v96, v97\n"
    "s_waitcnt lgkmcnt(1)\n"
    "v_mfma_f32_16x16x32_bf16 v[104:107], v[208:211], v[84:87], v[104:107]\n"
    "v_cvt_pk_bf16_f32 v161, v98, v99\n"
    "v_mfma_f32_16x16x32_bf16 v[108:111], v[212:215], v[84:87], v[108:111]\n"
    "v_cvt_pk_bf16_f32 v162, v100, v101\n"
    "s_waitcnt lgkmcnt(0)\n"
    "v_mfma_f32_16x16x32_bf16 v[104:107], v[216:219], v[92:95], v[104:107]\n"
    "v_cvt_pk_bf16_f32 v163, v102, v103\n"
    "v_mfma_f32_16x16x32_bf16 v[108:111], v[220:223], v[92:95], v[108:111]\n"
    "buffer_store_dwordx4 v[160:163], v2, s[28:31], s56 offen offset:0 sc1\n"
    "v_mfma_f32_16x16x32_bf16 v[112:115], v[224:227], v[64:67], 0\n"
    ".Lpad_2c_3:\n"
    "s_cbranch_execz .Lpadret_2c\n"
    "v_mfma_f32_16x16x32_bf16 v[116:119], v[228:231], v[64:67], 0\n"
    "s_nop 0\n"
    "v_mfma_f32_16x16x32_bf16 v[112:115], v[232:235], v[72:75], v[112:115]\n"
    "s_nop 0\n"
    "v_mfma_f32_16x16x32_bf16 v[116:119], v[236:239], v[72:75], v[116:119]\n"
    "v_cvt_pk_bf16_f32 v164, v104, v105\n"
    "v_mfma_f32_16x16x32_bf16 v[112:115], v[240:243], v[80:83], v[112:115]\n"
    "v_cvt_pk_bf16_f32 v165, v106, v107\n"
    "v_mfma_f32_16x16x32_bf16 v[116:119], v[244:247], v[80:83], v[116:119]\n"
    "v_cvt_pk_bf16_f32 v166, v108, v109\n"
    "v_mfma_f32_16x16x32_bf16 v[112:115], v[248:251], v[88:91], v[112:115]\n"
    "v_cvt_pk_bf16_f32 v167, v110, v111\n"
    "v_mfma_f32_16x16x32_bf16 v[116:119], v[252:255], v[88:91], v[116:119]\n"
    "buffer_store_dwordx4 v[164:167], v2, s[28:31], s56 offen offset:1024 sc1\n"
    "v_mfma_f32_16x16x32_bf16 v[120:123], v[224:227], v[68:71], 0\n"
    ".Lpad_2d_2:\n"
    "s_cbranch_execz .Lpad_2d_3\n"
    "v_mfma_f32_16x16x32_bf16 v[124:127], v[228:231], v[68:71], 0\n"
    "s_nop 0\n"
    "v_mfma_f32_16x16x32_bf16 v[120:123], v[232:235], v[76:79], v[120:123]\n"
    "s_nop 0\n"
    "v_mfma_f32_16x16x32_bf16 v[124:127], v[236:239], v[76:79], v[124:127]\n"
    "v_cvt_pk_bf16_f32 v192, v112, v113\n"
    "v_mfma_f32_16x16x32_bf16 v[120:123], v[240:243], v[84:87], v[120:123]\n"
    "v_cvt_pk_bf16_f32 v193, v114, v115\n"
    "v_mfma_f32_16x16x32_bf16 v[124:127], v[244:247], v[84:87], v[124:127]\n"
    "v_cvt_pk_bf16_f32 v194, v116, v117\n"
    "v_mfma_f32_16x16x32_bf16 v[120:123], v[248:251], v[92:95], v[120:123]\n"
    "v_cvt_pk_bf16_f32 v195, v118, v119\n"
    "v_mfma_f32_16x16x32_bf16 v[124:127], v[252:255], v[92:95], v[124:127]\n"
    "buffer_store_dwordx4 v[192:195], v2, s[28:31], s57 offen offset:0 sc1\n"
    ".Lpad_2d_3:\n"
    "s_cbranch_execz .Lpadret_2d\n"
    "s_nop 5\n"
    "v_cvt_pk_bf16_f32 v196, v120, v121\n"
    "v_cvt_pk_bf16_f32 v197, v122, v123\n"
    "v_cvt_pk_bf16_f32 v198, v124, v125\n"
    "v_cvt_pk_bf16_f32 v199, v126, v127\n"
    "buffer_store_dwordx4 v[196:199], v2, s[28:31], s57 offen offset:1024 sc1\n"
    "s_barrier\n"
    "s_mov_b64 exec, 1\n"
    "s_waitcnt vmcnt(6)\n"
    "buffer_atomic_swap_x2 v[4:5], off, s[28:31], s49 offset:0 sc1\n"
    "s_waitcnt vmcnt(5)\n"
    "buffer_atomic_swap_x2 v[6:7], off, s[28:31], s49 offset:1024 sc1\n"
    "s_waitcnt vmcnt(4)\n"
    "buffer_atomic_swap_x2 v[8:9], off, s[28:31], s49 offset:256 sc1\n"
    "s_waitcnt vmcnt(3)\n"
    "buffer_atomic_swap_x2 v[10:11], off, s[28:31], s49 offset:1280 sc1\n"
    "s_mov_b64 s[98:99], s[6:7]\n"
    "s_cmp_eq_u32 s41, 0\n"
    "s_cbranch_scc1 .Lpoller\n"
    "s_endpgm\n"
    ".Lpad_2b_5:\n"
    "s_cbranch_execz .Lpadret_2b\n"
    ".Lcode1:\n"
    :
    : "s"(__builtin_amdgcn_kernarg_segment_ptr()), "s"(blockIdx.x), "v"(threadIdx.x),
      "s"((unsigned)(size_t)(__attribute__((address_space(3))) unsigned char*)lds)
    : "v1", "v2", "v3", "v4", "v5", "v6", "v7", "v8", "v9", "v10", "v11", "v12", "v13", "v14", "v15", "v16", "v17", "v18", "v19", "v20", "v21", "v22", "v23", "v24", "v25", "v26", "v27", "v28", "v29", "v30", "v31", "v32", "v33", "v34", "v35", "v36", "v37", "v38", "v39", "v40", "v41", "v42", "v43", "v44", "v45", "v46", "v47", "v48", "v49", "v50", "v51", "v52", "v53", "v54", "v55", "v56", "v57", "v58", "v59", "v60", "v61", "v62", "v63", "v64", "v65", "v66", "v67", "v68", "v69", "v70", "v71", "v72", "v73", "v74", "v75", "v76", "v77", "v78", "v79", "v80", "v81", "v82", "v83", "v84", "v85", "v86", "v87", "v88", "v89", "v90", "v91", "v92", "v93", "v94", "v95", "v96", "v97", "v98", "v99", "v100", "v101", "v102", "v103", "v104", "v105", "v106", "v107", "v108", "v109", "v110", "v111", "v112", "v113", "v114", "v115", "v116", "v117", "v118", "v119", "v120", "v121", "v122", "v123", "v124", "v125", "v126", "v127", "v128", "v129", "v130", "v131", "v132", "v133", "v134", "v135", "v136", "v137", "v138", "v139", "v140", "v141", "v142", "v143", "v144", "v145", "v146", "v147", "v148", "v149", "v150", "v151", "v152", "v153", "v154", "v155", "v156", "v157", "v158", "v159", "v160", "v161", "v162", "v163", "v164", "v165", "v166", "v167", "v168", "v169", "v170", "v171", "v172", "v173", "v174", "v175", "v176", "v177", "v178", "v179", "v180", "v181", "v182", "v183", "v184", "v185", "v186", "v187", "v188", "v189", "v190", "v191", "v192", "v193", "v194", "v195", "v196", "v197", "v198", "v199", "v200", "v201", "v202", "v203", "v204", "v205", "v206", "v207", "v208", "v209", "v210", "v211", "v212", "v213", "v214", "v215", "v216", "v217", "v218", "v219", "v220", "v221", "v222", "v223", "v224", "v225", "v226", "v227", "v228", "v229", "v230", "v231", "v232", "v233", "v234", "v235", "v236", "v237", "v238", "v239", "v240", "v241", "v242", "v243", "v244", "v245", "v246", "v247", "v248", "v249", "v250", "v251", "v252", "v253", "v254", "v255", "s4", "s5", "s6", "s7", "s8", "s9", "s10", "s11", "s12", "s13", "s14", "s15", "s16", "s17", "s18", "s19", "s20", "s21", "s22", "s23", "s24", "s25", "s26", "s27", "s28", "s29", "s30", "s31", "s34", "s35", "s36", "s37", "s38", "s39", "s40", "s41", "s42", "s43", "s44", "s45", "s46", "s47", "s48", "s49", "s50", "s51", "s52", "s53", "s54", "s55", "s56", "s57", "s58", "s59", "s60", "s61", "s62", "s63", "s64", "s65", "s66", "s67", "s68", "s69", "s70", "s71", "s72", "s73", "s74", "s75", "s76", "s77", "s78", "s79", "s80", "s81", "s82", "s83", "s84", "s85", "s86", "s87", "s88", "s89", "s90", "s91", "s92", "s93", "s94", "s95", "s96", "s97", "s98", "s99", "vcc", "memory");
}

extern "C" void kernel_launch(void* const* d_in, const int* in_sizes, int n_in, void* d_out, int out_size,
                              void* d_ws, size_t ws_size, hipStream_t stream) {
  const unsigned F = 0x00020000u;
  gqa_block_kernel<<<256, 512, 0, stream>>>(
      d_in[0], (unsigned)in_sizes[0] * 2u, F, d_in[1], (unsigned)in_sizes[1] * 2u, F,
      d_in[2], (unsigned)in_sizes[2] * 2u, F, d_in[3], (unsigned)in_sizes[3] * 2u, F,
      d_in[4], (unsigned)in_sizes[4] * 2u, F, d_ws, 6291456u, F, d_out, (unsigned)out_size * 2u, F);
}
```
